```python
import torch

def _softmax(x, axis=-1):
    return torch.softmax(x, dim=axis)
def _gelu(x):
    return torch.nn.functional.gelu(x, approximate="tanh")
def _silu(x):
    return torch.nn.functional.silu(x)
def _sigmoid(x):
    return torch.sigmoid(x)
def _layernorm(x, axis=-1, eps=1e-5):
    m = x.mean(dim=axis, keepdim=True)
    v = x.var(dim=axis, keepdim=True, unbiased=False)
    return (x - m) / torch.sqrt(v + eps)
def _scatter_add(tgt, idx, src, axis=0):
    return tgt.clone().index_add_(axis, idx, src)
def _segment_sum(src, seg, n):
    out = torch.zeros((n,) + src.shape[1:], dtype=src.dtype)
    return out.index_add_(0, seg, src)
def _pad(x, pw):
    flat = tuple(v for pair in reversed(pw) for v in pair)
    return torch.nn.functional.pad(x, flat)

kernel_name = 'parallel_sum__linear_attn_block__mixer_channel_mlp__prompt'

def setup_inputs():

    _w = (lambda a: (lambda t: t.to(torch.bfloat16) if t.is_floating_point() else t)(torch.as_tensor(a)))
    def _A_setup():
        B, S, D = 1, 2048, 640
        return {
            "x": _w((lambda *s: torch.randn(*s, dtype=torch.bfloat16) * 0.05)(B, S, D)),
            "wqkv": _w((lambda *s: torch.randn(*s, dtype=torch.bfloat16) * 0.1)(D, 3 * D)),
            "wo": _w((lambda *s: torch.randn(*s, dtype=torch.bfloat16) * 0.05)(D, D)),
        }
    _A_in = _A_setup()

    def _B_setup():
        B, S, D, F = 1, 2048, 640, 1536
        return {
            "x": _w((lambda *s: torch.randn(*s, dtype=torch.bfloat16) * 0.05)(B, S, D)),
            "w1": _w((lambda *s: torch.randn(*s, dtype=torch.bfloat16) * 1.6)(D, F) * (D ** -0.5)),
            "b1": _w((lambda *s: torch.randn(*s, dtype=torch.bfloat16) * 0.8)(F)),
            "w2": _w((lambda *s: torch.randn(*s, dtype=torch.bfloat16) * 0.05)(F, D) * (F ** -0.5)),
            "b2": _w((lambda *s: torch.randn(*s, dtype=torch.bfloat16) * 0.05)(D)),
        }
    _B_in = _B_setup()

    return {
        "A_x": _A_in["x"],
        "A_wqkv": _A_in["wqkv"],
        "A_wo": _A_in["wo"],
        "B_x": _B_in["x"],
        "B_w1": _B_in["w1"],
        "B_b1": _B_in["b1"],
        "B_w2": _B_in["w2"],
        "B_b2": _B_in["b2"],
    }

def reference(A_x, A_wqkv, A_wo, B_x, B_w1, B_b1, B_w2, B_b2):
    def _A_ref(x, wqkv, wo):
        B, S, D = x.shape
        H = 32; Dh = D // H
        qkv = (lambda x, *d: x.permute(*d))((x @ wqkv).reshape(B, S, 3, H, Dh), 2, 0, 3, 1, 4)
        q, k, v = qkv[0], qkv[1], qkv[2]
        phi = lambda t: torch.where(t > 0, t + 1, torch.exp(t)) + 1e-6
        pq, pk = phi(q), phi(k)
        kv = torch.einsum("bhsd,bhse->bhde", pk, v)
        z = torch.einsum("bhsd->bhd", pk)
        num = torch.einsum("bhqd,bhde->bhqe", pq, kv)
        den = torch.einsum("bhqd,bhd->bhq", pq, z)[..., None] + 1e-6
        o = num / den
        return (lambda x, *d: x.permute(*d))(o, 0, 2, 1, 3).reshape(B, S, D) @ wo

    def _B_ref(x, w1, b1, w2, b2):
        y = _layernorm(x, axis=-1)
        h = _gelu(torch.einsum("bsd,df->bsf", y, w1) + b1)
        return torch.einsum("bsf,fd->bsd", h, w2) + b2

    x = A_x
    xn = _layernorm(x, axis=-1)
    return x + _A_ref(xn, A_wqkv, A_wo) + _B_ref(xn, B_w1, B_b1, B_w2, B_b2)
```

```cpp
#include <hip/hip_runtime.h>
#include <hip/hip_bf16.h>
#include <stdint.h>

typedef short s16x8 __attribute__((ext_vector_type(8)));
typedef short s16x4 __attribute__((ext_vector_type(4)));
typedef float f32x4 __attribute__((ext_vector_type(4)));
typedef unsigned u32x4 __attribute__((ext_vector_type(4)));
typedef unsigned u32x2 __attribute__((ext_vector_type(2)));
typedef __bf16 bf16x2v __attribute__((ext_vector_type(2)));
#define LAS __attribute__((address_space(3)))
#define GAS __attribute__((address_space(1)))
#define DI __device__ __forceinline__

namespace {

constexpr int KS_TOT = 68;
constexpr size_t ACT_BYTES = (size_t)128 * KS_TOT * 1024;
constexpr size_t WT_BYTES = (size_t)40 * KS_TOT * 1024;
constexpr size_t KVP_BYTES = (size_t)16 * 32 * 420 * 4;
constexpr size_t WS_ACT = 0;
constexpr size_t WS_WT = WS_ACT + ACT_BYTES;
constexpr size_t WS_KVP = WS_WT + WT_BYTES;
constexpr size_t WS_KVC = WS_KVP + KVP_BYTES;
constexpr unsigned SENT = 0xFFFFFFFFu;

constexpr int STG = 49152;
constexpr int L_RET = 3 * STG;
constexpr int L_STAT = L_RET + 12288;
constexpr int L_CS = L_STAT + 1536;
constexpr int K1_LDS = L_CS + 896;

DI unsigned pk_bf16(float a, float b) {
  bf16x2v v; v[0] = (__bf16)a; v[1] = (__bf16)b;
  return __builtin_bit_cast(unsigned, v);
}
DI float bf_lo(unsigned u) { return __uint_as_float(u << 16); }
DI float bf_hi(unsigned u) { return __uint_as_float(u & 0xffff0000u); }
DI float dot2(unsigned a, unsigned b, float c) {
  return __builtin_amdgcn_fdot2_f32_bf16(__builtin_bit_cast(bf16x2v, a), __builtin_bit_cast(bf16x2v, b), c, false);
}
DI float phi_f(float t) {
  float e = __builtin_amdgcn_exp2f(t * 1.4426950408889634f);
  return (t > 0.f ? t + 1.f : e) + 1e-6f;
}
DI float gelu_f(float v) {
  const float c1 = -2.f * 0.7978845608028654f * 1.4426950408889634f;
  const float c2 = c1 * 0.044715f;
  float p = __builtin_fmaf(v * v, c2, c1);
  float e = __builtin_amdgcn_exp2f(v * p);
  return v * __builtin_amdgcn_rcpf(1.f + e);
}
DI s16x8 tr_frag(const char* smem, unsigned off0, unsigned off1) {
  s16x4 lo = __builtin_amdgcn_ds_read_tr16_b64_v4i16((LAS s16x4*)(smem + off0));
  s16x4 hi = __builtin_amdgcn_ds_read_tr16_b64_v4i16((LAS s16x4*)(smem + off1));
  return __builtin_shufflevector(lo, hi, 0, 1, 2, 3, 4, 5, 6, 7);
}
#define MFMA16(a, b, c) __builtin_amdgcn_mfma_f32_16x16x32_bf16((a), (b), (c), 0, 0, 0)
#define GLDS(g, l) __builtin_amdgcn_global_load_lds((const GAS unsigned*)(g), (LAS unsigned*)(l), 16, 0, 0)

template <int RG, bool KV>
DI void k1_loop(char* smem, const char* xsrc, const char* wsrc, unsigned wstep, const unsigned (&aoff)[2],
                const unsigned (&boff)[4], unsigned adst, unsigned bdst, const unsigned (&ard)[2],
                const unsigned (&brd)[7], int hN, f32x4 (&acc)[2][7], float& s1, float& s2, float& cs0, float& cs1) {
  const unsigned ONES = 0x3F803F80u;
  auto stage = [&](int kt, unsigned sb) {
    const char* xs = xsrc + kt * 128;
    const char* wsb = wsrc + (size_t)kt * wstep;
    GLDS(xs + aoff[0], smem + sb + adst);
    GLDS(xs + aoff[1], smem + sb + adst + 1024);
#pragma unroll
    for (int i = 0; i < 4; ++i) GLDS(wsb + boff[i], smem + sb + bdst + i * 1024);
  };
  stage(0, 0);
  stage(1, STG);
  unsigned sb = 0;
#pragma unroll 1
  for (int kt = 0; kt < 10; ++kt) {
    if (kt < 9) asm volatile("s_waitcnt vmcnt(6)" ::: "memory");
    else asm volatile("s_waitcnt vmcnt(0)" ::: "memory");
    __builtin_amdgcn_s_barrier();
    asm volatile("" ::: "memory");
    if (kt + 2 < 10) {
      unsigned nb = sb + 2 * STG; if (nb >= 3 * STG) nb -= 3 * STG;
      stage(kt + 2, nb);
    }
#pragma unroll
    for (int s = 0; s < 2; ++s) {
      s16x8 a0 = *(const LAS s16x8*)(smem + sb + ard[s]);
      s16x8 a1 = *(const LAS s16x8*)(smem + sb + ard[s] + 2048);
      {
        u32x4 av = __builtin_bit_cast(u32x4, hN ? a1 : a0);
#pragma unroll
        for (int j = 0; j < 4; ++j) { s1 = dot2(av[j], ONES, s1); s2 = dot2(av[j], av[j], s2); }
      }
#pragma unroll
      for (int u = 0; u < 7; ++u) {
        s16x8 b = tr_frag(smem, sb + brd[u] + s * 16384, sb + brd[u] + s * 16384 + 2048);
        if (u == RG || u == RG + 4) {
          u32x4 bv = __builtin_bit_cast(u32x4, b);
          float c = (u == RG) ? cs0 : cs1;
#pragma unroll
          for (int j = 0; j < 4; ++j) c = dot2(bv[j], ONES, c);
          if (u == RG) cs0 = c; else cs1 = c;
        }
        if (KV && u < 5) {
          acc[0][u] = MFMA16(a0, b, acc[0][u]);
          acc[1][u] = MFMA16(a1, b, acc[1][u]);
        } else {
          acc[0][u] = MFMA16(b, a0, acc[0][u]);
          acc[1][u] = MFMA16(b, a1, acc[1][u]);
        }
      }
    }
    sb += STG; if (sb >= 3 * STG) sb = 0;
  }
}

template <bool KV>
DI void k1_dispatch(int rg, char* smem, const char* xsrc, const char* wsrc, unsigned wstep, const unsigned (&aoff)[2],
                    const unsigned (&boff)[4], unsigned adst, unsigned bdst, const unsigned (&ard)[2],
                    const unsigned (&brd)[7], int hN, f32x4 (&acc)[2][7], float& s1, float& s2, float& cs0, float& cs1) {
  if (rg == 0) k1_loop<0, KV>(smem, xsrc, wsrc, wstep, aoff, boff, adst, bdst, ard, brd, hN, acc, s1, s2, cs0, cs1);
  else if (rg == 1) k1_loop<1, KV>(smem, xsrc, wsrc, wstep, aoff, boff, adst, bdst, ard, brd, hN, acc, s1, s2, cs0, cs1);
  else if (rg == 2) k1_loop<2, KV>(smem, xsrc, wsrc, wstep, aoff, boff, adst, bdst, ard, brd, hN, acc, s1, s2, cs0, cs1);
  else k1_loop<3, KV>(smem, xsrc, wsrc, wstep, aoff, boff, adst, bdst, ard, brd, hN, acc, s1, s2, cs0, cs1);
}

__global__ __launch_bounds__(512, 2) void k1(const char* __restrict__ x, const char* __restrict__ wqkv,
                                            const char* __restrict__ wo, const char* __restrict__ w1,
                                            const char* __restrict__ b1, const char* __restrict__ w2,
                                            char* __restrict__ ws) {
  __shared__ __attribute__((aligned(16))) char smem[K1_LDS];
  const int tid = threadIdx.x, lane = tid & 63;
  const int w = __builtin_amdgcn_readfirstlane(tid >> 6);
  const int rg = w & 3, hN = w >> 2;
  const int bid = blockIdx.x;
  const int xcd = bid & 7, idx = bid >> 3;
  const int rb = (xcd & 1) * 8 + (idx & 7);
  const int ct = (xcd >> 1) * 4 + (idx >> 3);
  const bool kvt = ct < 8;
  const int g = lane >> 4, l15 = lane & 15, q4 = l15 >> 2, p4 = lane & 3;

  const int piece = bid * 3 + w;
  const bool do_ret = (w < 3) && (piece < 680);
  const int pks = piece / 10, pcg = piece % 10;
  if (do_ret) {
    const char* src = (pks < 48) ? (w2 + (size_t)pks * 32 * 1280) : (wo + (size_t)(pks - 48) * 32 * 1280);
    src += pcg * 128 + (lane >> 3) * 1280 + (lane & 7) * 16;
#pragma unroll
    for (int i = 0; i < 4; ++i) GLDS(src + i * 8 * 1280, smem + L_RET + w * 4096 + i * 1024);
  }

  unsigned aoff[2], boff[4], ard[2], brd[7];
#pragma unroll
  for (int i = 0; i < 2; ++i) {
    int row = 16 * w + 8 * i + (lane >> 3);
    int f = (row >> 1) & 7;
    aoff[i] = (unsigned)(row * 1280 + (((lane & 7) ^ f) << 4));
  }
  const char* wsrc; unsigned ldb;
  int kch0 = 0, hch0 = 0;
  int kws = 0, vws = 0, hL = 0;
  if (ct < 9) { wsrc = wqkv; ldb = 3840; } else { wsrc = w1; ldb = 3072; }
  if (kvt) {
    int kb = 1280 + 160 * ct, L = kb & ~127;
    kws = L + min(kb - L, 64); vws = kws + 1280; kch0 = (kb - kws) >> 4;
  } else if (ct > 8) {
    int hb = 448 * (ct - 9); hL = hb & ~127; hch0 = (hb - hL) >> 4;
  }
#pragma unroll
  for (int i = 0; i < 4; ++i) {
    int row = 8 * w + 2 * i + (lane >> 5);
    int s = (row & 3) | (((row >> 3) & 1) << 2);
    int ch = (lane & 31) ^ (s << 1);
    int cb;
    if (kvt) cb = ch < 12 ? kws + 16 * ch : (ch < 24 ? vws + 16 * (ch - 12) : 128 * ct + 16 * (ch - 24));
    else if (ct == 8) cb = 1024 + 16 * (ch & 15);
    else { cb = hL + 16 * ch; if (cb >= 3072) cb -= 128; }
    boff[i] = (unsigned)(row * ldb + cb);
  }
  {
    int row = 32 * rg + l15, f = (row >> 1) & 7;
    ard[0] = (unsigned)(row * 128 + ((g ^ f) << 4));
    ard[1] = ard[0] ^ 64u;
    int sx = q4 | ((g & 1) << 2);
#pragma unroll
    for (int u = 0; u < 7; ++u) {
      int xx = 16 * u + 4 * p4, c4;
      if (kvt) {
        if (xx < 40) c4 = 2 * kch0 + (40 * hN + xx) / 4;
        else if (xx < 80) c4 = 2 * (12 + kch0) + (40 * hN + xx - 40) / 4;
        else c4 = 48 + (32 * hN + xx - 80) / 4;
      } else if (ct == 8) c4 = ((64 * hN + xx) / 4) & 31;
      else c4 = (2 * hch0 + (112 * hN + xx) / 4) & 63;
      int ch = c4 >> 1, hf = c4 & 1;
      brd[u] = (unsigned)(16384 + (8 * g + q4) * 512 + ((ch ^ (sx << 1)) << 4) + hf * 8);
    }
  }
  const char* xsrc = x + (size_t)rb * 128 * 1280;
  const unsigned adst = (unsigned)(16 * w * 128), bdst = (unsigned)(16384 + 8 * w * 512);

  f32x4 acc[2][7];
#pragma unroll
  for (int i = 0; i < 2; ++i)
#pragma unroll
    for (int u = 0; u < 7; ++u) acc[i][u] = (f32x4){0.f, 0.f, 0.f, 0.f};
  float s1 = 0.f, s2 = 0.f, cs0 = 0.f, cs1 = 0.f;

  if (kvt) k1_dispatch<true>(rg, smem, xsrc, wsrc, 64u * ldb, aoff, boff, adst, bdst, ard, brd, hN, acc, s1, s2, cs0, cs1);
  else k1_dispatch<false>(rg, smem, xsrc, wsrc, 64u * ldb, aoff, boff, adst, bdst, ard, brd, hN, acc, s1, s2, cs0, cs1);

  float* st_m = (float*)(smem + L_STAT);
  float* st_r = st_m + 128;
  float* st_rr = st_m + 256;
  float* csum = (float*)(smem + L_CS);
  s1 += __shfl_xor(s1, 16); s1 += __shfl_xor(s1, 32);
  s2 += __shfl_xor(s2, 16); s2 += __shfl_xor(s2, 32);
  cs0 += __shfl_xor(cs0, 16); cs0 += __shfl_xor(cs0, 32);
  cs1 += __shfl_xor(cs1, 16); cs1 += __shfl_xor(cs1, 32);
  if (g == 0) {
    float m = s1 * (1.f / 640.f);
    float var = fmaxf(s2 * (1.f / 640.f) - m * m, 0.f);
    float r = __builtin_amdgcn_rsqf(var + 1e-5f);
    float v2 = var * r * r;
    float r2 = __builtin_amdgcn_rsqf(v2 + 1e-5f);
    int row = 32 * rg + 16 * hN + l15;
    st_m[row] = m; st_r[row] = r; st_rr[row] = r * r2;
    csum[hN * 112 + rg * 16 + l15] = cs0;
    if (rg < 3) csum[hN * 112 + (rg + 4) * 16 + l15] = cs1;
  }
  __syncthreads();
  float* kvacc = (float*)smem;
  if (kvt) {
    for (int i = tid; i < 1680; i += 512) kvacc[i] = 0.f;
  }

  if (do_ret) {
    char* dst = ws + WS_WT + (size_t)pks * 1024 + lane * 16;
    unsigned base = (unsigned)(L_RET + w * 4096 + (4 * g + q4) * 128 + p4 * 8);
#pragma unroll
    for (int n = 0; n < 4; ++n) {
      s16x8 f = tr_frag(smem, base + n * 32, base + n * 32 + 2048);
      *(s16x8*)(dst + (size_t)(pcg * 4 + n) * KS_TOT * 1024) = f;
    }
  }
  if (bid < 8) ((u32x4*)(ws + WS_KVC))[bid * 256 + (tid & 255)] = (u32x4){SENT, SENT, SENT, SENT};

  char* act = ws + WS_ACT;
  const int ubeg = kvt ? 5 : 0;
  float mS[2], sc[2];
#pragma unroll
  for (int rt = 0; rt < 2; ++rt) {
    int row = 32 * rg + 16 * rt + l15;
    float m = st_m[row];
    sc[rt] = (ct > 8) ? st_rr[row] : st_r[row];
    mS[rt] = -m * sc[rt];
  }
#pragma unroll
  for (int u = 0; u < 7; ++u) {
    if (u < ubeg) continue;
    int U;
    bool valid;
    if (kvt) { U = 96 + 4 * ct + 2 * hN + (u - 5); valid = true; }
    else if (ct == 8) { U = 96 + 32 + 4 * hN + u; valid = u < 4; }
    else { U = 14 * (ct - 9) + 7 * hN + u; valid = U < 96; }
    if (!valid) continue;
    f32x4 c = *(const f32x4*)(csum + hN * 112 + u * 16 + 4 * g);
    f32x4 bias = (f32x4){0.f, 0.f, 0.f, 0.f};
    if (ct > 8) {
      u32x2 bb = *(const u32x2*)(b1 + (size_t)(U * 16 + 4 * g) * 2);
      bias = (f32x4){bf_lo(bb[0]), bf_hi(bb[0]), bf_lo(bb[1]), bf_hi(bb[1])};
    }
#pragma unroll
    for (int rt = 0; rt < 2; ++rt) {
      float o[4];
#pragma unroll
      for (int i = 0; i < 4; ++i) {
        float v = __builtin_fmaf(acc[rt][u][i], sc[rt], __builtin_fmaf(mS[rt], c[i], bias[i]));
        o[i] = (ct > 8) ? gelu_f(v) : phi_f(v);
      }
      u32x2 pk = (u32x2){pk_bf16(o[0], o[1]), pk_bf16(o[2], o[3])};
      int rtile = rb * 8 + rg * 2 + rt;
      *(u32x2*)(act + ((size_t)rtile * KS_TOT + (U >> 1)) * 1024 + lane * 16 + (U & 1) * 8) = pk;
    }
  }

  if (kvt) {
    f32x4 mv[2], rv[2];
#pragma unroll
    for (int rt = 0; rt < 2; ++rt) {
      mv[rt] = *(const f32x4*)(st_m + 32 * rg + 16 * rt + 4 * g);
      rv[rt] = *(const f32x4*)(st_r + 32 * rg + 16 * rt + 4 * g);
    }
    s16x8 fr[5];
#pragma unroll
    for (int u = 0; u < 5; ++u) {
      float c = csum[hN * 112 + u * 16 + l15];
      bool isk = (u < 2) || (u == 2 && l15 < 8);
      unsigned pw[4];
#pragma unroll
      for (int rt = 0; rt < 2; ++rt) {
        float o[4];
#pragma unroll
        for (int i = 0; i < 4; ++i) {
          float v = (acc[rt][u][i] - mv[rt][i] * c) * rv[rt][i];
          o[i] = (u < 3) ? (isk ? phi_f(v) : v) : v;
        }
        pw[2 * rt] = pk_bf16(o[0], o[1]); pw[2 * rt + 1] = pk_bf16(o[2], o[3]);
      }
      fr[u] = __builtin_bit_cast(s16x8, (u32x4){pw[0], pw[1], pw[2], pw[3]});
    }
    const s16x8 ones = __builtin_bit_cast(s16x8, (u32x4){0x3F803F80u, 0x3F803F80u, 0x3F803F80u, 0x3F803F80u});
    __syncthreads();
    const f32x4 z4 = (f32x4){0.f, 0.f, 0.f, 0.f};
    auto emit = [&](f32x4 d, int ua, int ub) {
      int dk = 16 * ua + 4 * g;
      int ev = (ub < 0) ? 0 : 16 * ub + l15 - 40;
      if (dk < 40 && ev >= 0) {
        int hd = dk / 20, he = (ub < 0) ? hd : ev / 20;
        bool ok = (ub < 0) ? (l15 == 0) : (hd == he);
        if (ok) {
          int erow = (ub < 0) ? 20 : ev - 20 * he;
          float* dstp = kvacc + (2 * hN + hd) * 420 + erow * 20 + (dk - 20 * hd);
#pragma unroll
          for (int i = 0; i < 4; ++i) __hip_atomic_fetch_add(dstp + i, d[i], __ATOMIC_RELAXED, __HIP_MEMORY_SCOPE_WORKGROUP);
        }
      }
    };
    emit(MFMA16(fr[0], fr[2], z4), 0, 2);
    emit(MFMA16(fr[0], fr[3], z4), 0, 3);
    emit(MFMA16(fr[1], fr[2], z4), 1, 2);
    emit(MFMA16(fr[1], fr[3], z4), 1, 3);
    emit(MFMA16(fr[1], fr[4], z4), 1, 4);
    emit(MFMA16(fr[2], fr[3], z4), 2, 3);
    emit(MFMA16(fr[2], fr[4], z4), 2, 4);
    emit(MFMA16(fr[0], ones, z4), 0, -1);
    emit(MFMA16(fr[1], ones, z4), 1, -1);
    emit(MFMA16(fr[2], ones, z4), 2, -1);
    __syncthreads();
    float* kvp = (float*)(ws + WS_KVP) + ((size_t)rb * 32 + 4 * ct) * 420;
    if (tid < 420) ((f32x4*)kvp)[tid] = ((const f32x4*)kvacc)[tid];
  }
}

constexpr int K2_KV = 0;
constexpr int K2_ZERO = 32768;
constexpr int K2_RED = 33024;
constexpr int K2_LDS = K2_RED + 61440;
constexpr int DEPTH = 4;

__global__ __launch_bounds__(256, 1) void k2(const char* __restrict__ x, const char* __restrict__ b2,
                                            char* __restrict__ ws, char* __restrict__ out) {
  __shared__ __attribute__((aligned(16))) char smem[K2_LDS];
  const int tid = threadIdx.x, lane = tid & 63;
  const int w = __builtin_amdgcn_readfirstlane(tid >> 6);
  const int bid = blockIdx.x;
  const int xcd = bid & 7, idx = bid >> 3;
  const int rtl = (xcd >> 1) * 8 + (idx & 7);
  const int cl = (xcd & 1) * 4 + (idx >> 3);
  const int g = lane >> 4, l15 = lane & 15;
  const char* act = ws + WS_ACT + (size_t)rtl * 4 * KS_TOT * 1024 + lane * 16;
  const char* wt = ws + WS_WT + (size_t)cl * 5 * KS_TOT * 1024 + lane * 16;

  float2 part[16];
  const int rhead = bid >> 3, rdw = (bid & 7) * 27 + lane;
  const bool red = (w == 0) && (lane < 27) && (rdw < 210);
  if (red) {
    const float* src = (const float*)(ws + WS_KVP) + (size_t)rhead * 420 + rdw * 2;
#pragma unroll
    for (int p = 0; p < 16; ++p) part[p] = *(const float2*)(src + (size_t)p * 32 * 420);
  }
  if (tid < 4) *(unsigned*)(smem + K2_ZERO + tid * 4) = 0u;

  s16x8 fa[DEPTH][4], fb[DEPTH][5];
  auto loadstep = [&](int slot, int ks) {
#pragma unroll
    for (int r = 0; r < 4; ++r) fa[slot][r] = *(const s16x8*)(act + ((size_t)r * KS_TOT + ks) * 1024);
#pragma unroll
    for (int n = 0; n < 5; ++n) fb[slot][n] = *(const s16x8*)(wt + ((size_t)n * KS_TOT + ks) * 1024);
  };
  const int ks0 = 12 * w;
#pragma unroll
  for (int s = 0; s < DEPTH; ++s) loadstep(s, ks0 + s);

  if (red) {
    float a = 0.f, b = 0.f;
#pragma unroll
    for (int p = 0; p < 16; ++p) { a += part[p].x; b += part[p].y; }
    unsigned v = pk_bf16(a, b);
    if (v == SENT) v = 0x7FC07FC0u;
    __hip_atomic_store((GAS unsigned*)(ws + WS_KVC) + rhead * 256 + rdw, v, __ATOMIC_RELAXED, __HIP_MEMORY_SCOPE_AGENT);
  }

  f32x4 acc[5][4];
#pragma unroll
  for (int n = 0; n < 5; ++n)
#pragma unroll
    for (int r = 0; r < 4; ++r) acc[n][r] = (f32x4){0.f, 0.f, 0.f, 0.f};

  s16x8 pq[4][5], wof[5][5];
  const int kso = 48 + 5 * w;
#pragma unroll
  for (int s = 0; s < 12; ++s) {
    const int slot = s % DEPTH;
    s16x8 ca[4], cb[5];
#pragma unroll
    for (int r = 0; r < 4; ++r) ca[r] = fa[slot][r];
#pragma unroll
    for (int n = 0; n < 5; ++n) cb[n] = fb[slot][n];
    if (s + DEPTH < 12) loadstep(slot, ks0 + s + DEPTH);
    else {
      const int b = s + DEPTH - 12;
#pragma unroll
      for (int bb = 0; bb < 5; ++bb) {
        if ((bb % DEPTH) == b) {
#pragma unroll
          for (int r = 0; r < 4; ++r) pq[r][bb] = *(const s16x8*)(act + ((size_t)r * KS_TOT + kso + bb) * 1024);
#pragma unroll
          for (int n = 0; n < 5; ++n) wof[bb][n] = *(const s16x8*)(wt + ((size_t)n * KS_TOT + kso + bb) * 1024);
        }
      }
    }
#pragma unroll
    for (int n = 0; n < 5; ++n)
#pragma unroll
      for (int r = 0; r < 4; ++r) acc[n][r] = MFMA16(cb[n], ca[r], acc[n][r]);
  }

  {
    const __amdgpu_buffer_rsrc_t rs = __builtin_amdgcn_make_buffer_rsrc((void*)(ws + WS_KVC), 0, 32768, 0x00020000);
    u32x4 kvv[8];
    for (int spin = 0; spin < (1 << 20); ++spin) {
      bool bad = false;
#pragma unroll
      for (int h = 0; h < 8; ++h) kvv[h] = __builtin_amdgcn_raw_buffer_load_b128(rs, (unsigned)((8 * w + h) * 1024 + lane * 16), 0, 16);
#pragma unroll
      for (int h = 0; h < 8; ++h) {
        bool b01 = (kvv[h][0] == SENT) || (kvv[h][1] == SENT);
        bool b23 = (kvv[h][2] == SENT) || (kvv[h][3] == SENT);
        bad |= (lane < 52) ? (b01 || b23) : ((lane == 52) ? b01 : false);
      }
      if (!__any(bad)) break;
      __builtin_amdgcn_s_sleep(2);
    }
#pragma unroll
    for (int h = 0; h < 8; ++h) *(u32x4*)(smem + K2_KV + w * 8192 + h * 1024 + lane * 16) = kvv[h];
  }
  __syncthreads();

  {
    const char* kvb = smem + K2_KV + w * 8192;
    auto kvfrag = [&](int he, int ebase, int b) -> s16x8 {
      u32x2 h2[2];
#pragma unroll
      for (int hh = 0; hh < 2; ++hh) {
        int d = 32 * b + 16 * hh + 4 * g;
        int hd = d / 20;
        const char* p = (hd == he) ? (kvb + ebase + (d - 20 * hd) * 2) : (smem + K2_ZERO);
        h2[hh] = *(const u32x2*)p;
      }
      return __builtin_bit_cast(s16x8, (u32x4){h2[0][0], h2[0][1], h2[1][0], h2[1][1]});
    };
    const f32x4 z4 = (f32x4){0.f, 0.f, 0.f, 0.f};
    f32x4 den[2][4];
#pragma unroll
    for (int c = 0; c < 2; ++c) {
      int he = 4 * c + (l15 & 3);
      int eb = he * 1024 + 800;
#pragma unroll
      for (int r = 0; r < 4; ++r) den[c][r] = z4;
#pragma unroll
      for (int bb = 0; bb < 3; ++bb) {
        s16x8 zf = kvfrag(he, eb, 2 * c + bb);
#pragma unroll
        for (int r = 0; r < 4; ++r) den[c][r] = MFMA16(zf, pq[r][2 * c + bb], den[c][r]);
      }
#pragma unroll
      for (int r = 0; r < 4; ++r)
#pragma unroll
        for (int i = 0; i < 4; ++i) den[c][r][i] = __builtin_amdgcn_rcpf(den[c][r][i] + 1e-6f);
    }
#pragma unroll
    for (int bp = 0; bp < 5; ++bp) {
      unsigned ow[4][4];
#pragma unroll
      for (int hf = 0; hf < 2; ++hf) {
        const int jt = 2 * bp + hf;
        int e = 16 * jt + l15, he = e / 20, eb = he * 1024 + (e - 20 * he) * 40;
        const int dlo = 20 * ((16 * jt) / 20), dhi = 20 * ((16 * jt + 15) / 20) + 19;
        const int blo = dlo / 32, bhi = dhi / 32;
        f32x4 num[4];
#pragma unroll
        for (int r = 0; r < 4; ++r) num[r] = z4;
#pragma unroll
        for (int b = 0; b < 5; ++b) {
          if (b < blo || b > bhi) continue;
          s16x8 kf = kvfrag(he, eb, b);
#pragma unroll
          for (int r = 0; r < 4; ++r) num[r] = MFMA16(kf, pq[r][b], num[r]);
        }
        const int h0 = (4 * jt) / 5, h1 = (4 * jt + 3) / 5;
        const bool hi = g >= 5 * h1 - 4 * jt;
#pragma unroll
        for (int r = 0; r < 4; ++r) {
          float rd = den[h0 >> 2][r][h0 & 3];
          if (h1 != h0) rd = hi ? den[h1 >> 2][r][h1 & 3] : rd;
          ow[r][2 * hf] = pk_bf16(num[r][0] * rd, num[r][1] * rd);
          ow[r][2 * hf + 1] = pk_bf16(num[r][2] * rd, num[r][3] * rd);
        }
      }
#pragma unroll
      for (int r = 0; r < 4; ++r) {
        s16x8 of = __builtin_bit_cast(s16x8, (u32x4){ow[r][0], ow[r][1], ow[r][2], ow[r][3]});
#pragma unroll
        for (int n = 0; n < 5; ++n) acc[n][r] = MFMA16(wof[bp][n], of, acc[n][r]);
      }
    }
  }

  {
    char* red = smem + K2_RED;
#pragma unroll
    for (int r = 0; r < 4; ++r) {
      if (r == w) continue;
      int slot = (w < r) ? w : w - 1;
#pragma unroll
      for (int n = 0; n < 5; ++n) *(f32x4*)(red + ((r * 3 + slot) * 5 + n) * 1024 + lane * 16) = acc[n][r];
    }
    __syncthreads();
    const int row = rtl * 64 + w * 16 + l15;
#pragma unroll
    for (int n = 0; n < 5; ++n) {
      f32x4 v = (w == 0) ? acc[n][0] : (w == 1) ? acc[n][1] : (w == 2) ? acc[n][2] : acc[n][3];
#pragma unroll
      for (int sidx = 0; sidx < 3; ++sidx) v += *(const f32x4*)(red + ((w * 3 + sidx) * 5 + n) * 1024 + lane * 16);
      const int col = cl * 80 + n * 16 + 4 * g;
      u32x2 xv = *(const u32x2*)(x + ((size_t)row * 640 + col) * 2);
      u32x2 bv = *(const u32x2*)(b2 + col * 2);
      float o0 = v[0] + bf_lo(xv[0]) + bf_lo(bv[0]);
      float o1 = v[1] + bf_hi(xv[0]) + bf_hi(bv[0]);
      float o2 = v[2] + bf_lo(xv[1]) + bf_lo(bv[1]);
      float o3 = v[3] + bf_hi(xv[1]) + bf_hi(bv[1]);
      *(u32x2*)(out + ((size_t)row * 640 + col) * 2) = (u32x2){pk_bf16(o0, o1), pk_bf16(o2, o3)};
    }
  }
}

}

extern "C" void kernel_launch(void* const* d_in, const int* in_sizes, int n_in,
                              void* d_out, int out_size, void* d_ws, size_t ws_size,
                              hipStream_t stream) {
  const char* x = (const char*)d_in[0];
  const char* wqkv = (const char*)d_in[1];
  const char* wo = (const char*)d_in[2];
  const char* w1 = (const char*)d_in[4];
  const char* b1 = (const char*)d_in[5];
  const char* w2 = (const char*)d_in[6];
  const char* b2 = (const char*)d_in[7];
  k1<<<256, 512, 0, stream>>>(x, wqkv, wo, w1, b1, w2, (char*)d_ws);
  k2<<<256, 256, 0, stream>>>(x, b2, (char*)d_ws, (char*)d_out);
}
```

```cpp
#include <hip/hip_runtime.h>
#include <hip/hip_bf16.h>
#include <stdint.h>

typedef short s16x8 __attribute__((ext_vector_type(8)));
typedef short s16x4 __attribute__((ext_vector_type(4)));
typedef float f32x4 __attribute__((ext_vector_type(4)));
typedef unsigned u32x4 __attribute__((ext_vector_type(4)));
typedef unsigned u32x2 __attribute__((ext_vector_type(2)));
typedef __bf16 bf16x2v __attribute__((ext_vector_type(2)));
#define LAS __attribute__((address_space(3)))
#define GAS __attribute__((address_space(1)))
#define DI __device__ __forceinline__

namespace {

template <int V> struct IC { static constexpr int value = V; };

constexpr int KS_TOT = 68;
constexpr size_t ACT_BYTES = (size_t)128 * KS_TOT * 1024;
constexpr size_t WT_BYTES = (size_t)40 * KS_TOT * 1024;
constexpr size_t KVP_BYTES = (size_t)16 * 32 * 420 * 4;
constexpr size_t WS_ACT = 0;
constexpr size_t WS_WT = WS_ACT + ACT_BYTES;
constexpr size_t WS_KVP = WS_WT + WT_BYTES;
constexpr size_t WS_KVC = WS_KVP + KVP_BYTES;
constexpr unsigned SENT = 0xFFFFFFFFu;

constexpr int STG = 49152;
constexpr int L_STAT = 3 * STG;
constexpr int L_CS = L_STAT + 1536;
constexpr int K1_LDS = L_CS + 896;

DI unsigned pk_bf16(float a, float b) {
  bf16x2v v; v[0] = (__bf16)a; v[1] = (__bf16)b;
  return __builtin_bit_cast(unsigned, v);
}
DI float bf_lo(unsigned u) { return __uint_as_float(u << 16); }
DI float bf_hi(unsigned u) { return __uint_as_float(u & 0xffff0000u); }
DI float dot2(unsigned a, unsigned b, float c) {
  return __builtin_amdgcn_fdot2_f32_bf16(__builtin_bit_cast(bf16x2v, a), __builtin_bit_cast(bf16x2v, b), c, false);
}
DI float phi_f(float t) {
  float e = __builtin_amdgcn_exp2f(t * 1.4426950408889634f);
  return (t > 0.f ? t + 1.f : e) + 1e-6f;
}
DI float gelu_f(float v) {
  const float c1 = -2.f * 0.7978845608028654f * 1.4426950408889634f;
  const float c2 = c1 * 0.044715f;
  float p = __builtin_fmaf(v * v, c2, c1);
  float e = __builtin_amdgcn_exp2f(v * p);
  return v * __builtin_amdgcn_rcpf(1.f + e);
}
DI s16x8 tr_frag(const char* smem, unsigned off0, unsigned off1) {
  s16x4 lo = __builtin_amdgcn_ds_read_tr16_b64_v4i16((LAS s16x4*)(smem + off0));
  s16x4 hi = __builtin_amdgcn_ds_read_tr16_b64_v4i16((LAS s16x4*)(smem + off1));
  return __builtin_shufflevector(lo, hi, 0, 1, 2, 3, 4, 5, 6, 7);
}
#define MFMA16(a, b, c) __builtin_amdgcn_mfma_f32_16x16x32_bf16((a), (b), (c), 0, 0, 0)
DI void glds_s(const char* sbase, unsigned voff, unsigned ldst) {
  unsigned keep;
  asm volatile("s_mov_b32 %0, m0\n\ts_mov_b32 m0, %3\n\ts_nop 0\n\tglobal_load_lds_dwordx4 %1, %2\n\ts_mov_b32 m0, %0"
               : "=&s"(keep) : "v"(voff), "s"(sbase), "s"(ldst) : "memory");
}
#define WAIT_VM(n) asm volatile("s_waitcnt vmcnt(" #n ")" ::: "memory")
#define BARRIER() do { asm volatile("" ::: "memory"); __builtin_amdgcn_s_barrier(); asm volatile("" ::: "memory"); } while (0)
#define STAMP(i)

template <int RG, bool KV>
DI void k1_loop(const char* smem, const unsigned (&ard)[2], const unsigned (&brd)[7], int hN,
                f32x4 (&acc)[2][7], float& s1, float& s2, float& cs0, float& cs1) {
  const unsigned ONES = 0x3F803F80u;
  unsigned sb = 0;
#pragma unroll 1
  for (int kt = 0; kt < 10; ++kt) {
    BARRIER();
#pragma unroll
    for (int s = 0; s < 2; ++s) {
      s16x8 a0 = *(const LAS s16x8*)(smem + sb + ard[s]);
      s16x8 a1 = *(const LAS s16x8*)(smem + sb + ard[s] + 2048);
      {
        u32x4 av = __builtin_bit_cast(u32x4, hN ? a1 : a0);
#pragma unroll
        for (int j = 0; j < 4; ++j) { s1 = dot2(av[j], ONES, s1); s2 = dot2(av[j], av[j], s2); }
      }
#pragma unroll
      for (int u = 0; u < 7; ++u) {
        s16x8 b = tr_frag(smem, sb + brd[u] + s * 16384, sb + brd[u] + s * 16384 + 2048);
        if (u == RG || u == RG + 4) {
          u32x4 bv = __builtin_bit_cast(u32x4, b);
          float c = (u == RG) ? cs0 : cs1;
#pragma unroll
          for (int j = 0; j < 4; ++j) c = dot2(bv[j], ONES, c);
          if (u == RG) cs0 = c; else cs1 = c;
        }
        if (KV && u < 5) {
          acc[0][u] = MFMA16(a0, b, acc[0][u]);
          acc[1][u] = MFMA16(a1, b, acc[1][u]);
        } else {
          acc[0][u] = MFMA16(b, a0, acc[0][u]);
          acc[1][u] = MFMA16(b, a1, acc[1][u]);
        }
      }
    }
    sb += STG; if (sb >= 3 * STG) sb = 0;
  }
}

template <int TYPE>
DI void k1_compute(char* smem, const char* __restrict__ b1, char* __restrict__ ws, int w, int lane, int rb, int ct, int ch0) {
  constexpr int KID = 0;
  const int rg = w & 3, hN = w >> 2;
  const int g = lane >> 4, l15 = lane & 15, q4 = l15 >> 2, p4 = lane & 3;
  unsigned ard[2], brd[7];
  {
    int row = 32 * rg + l15, f = (row >> 1) & 7;
    ard[0] = (unsigned)(row * 128 + ((g ^ f) << 4));
    ard[1] = ard[0] ^ 64u;
    int sx = q4 | ((g & 1) << 2);
#pragma unroll
    for (int u = 0; u < 7; ++u) {
      int xx = 16 * u + 4 * p4, c4;
      if (TYPE == 0) {
        int ck = 2 * ch0 + 10 * hN + 4 * u + p4;
        int cv = 2 * (12 + ch0) + 10 * hN + 4 * u + p4 - 10;
        int cq = 48 + 8 * hN + 4 * u + p4 - 20;
        c4 = (u < 2) ? ck : (u == 2) ? (p4 < 2 ? ck : cv) : (u < 5) ? cv : cq;
      } else if (TYPE == 1) c4 = ((64 * hN + xx) / 4) & 31;
      else c4 = (2 * ch0 + (112 * hN + xx) / 4) & 63;
      int ch = c4 >> 1, hf = c4 & 1;
      brd[u] = (unsigned)(16384 + (8 * g + q4) * 512 + ((ch ^ (sx << 1)) << 4) + hf * 8);
    }
  }
  const int U0 = (TYPE == 0) ? 96 + 4 * ct + 2 * hN - 5 : (TYPE == 1) ? 128 + 4 * hN : 14 * (ct - 9) + 7 * hN;
  u32x2 bias[7];
  if (TYPE == 2) {
#pragma unroll
    for (int u = 0; u < 7; ++u) bias[u] = *(const u32x2*)(b1 + (size_t)(min(U0 + u, 95) * 16 + 4 * g) * 2);
  }

  f32x4 acc[2][7];
#pragma unroll
  for (int i = 0; i < 2; ++i)
#pragma unroll
    for (int u = 0; u < 7; ++u) acc[i][u] = (f32x4){0.f, 0.f, 0.f, 0.f};
  float s1 = 0.f, s2 = 0.f, cs0 = 0.f, cs1 = 0.f;
  STAMP(1);
  if (rg == 0) k1_loop<0, TYPE == 0>(smem, ard, brd, hN, acc, s1, s2, cs0, cs1);
  else if (rg == 1) k1_loop<1, TYPE == 0>(smem, ard, brd, hN, acc, s1, s2, cs0, cs1);
  else if (rg == 2) k1_loop<2, TYPE == 0>(smem, ard, brd, hN, acc, s1, s2, cs0, cs1);
  else k1_loop<3, TYPE == 0>(smem, ard, brd, hN, acc, s1, s2, cs0, cs1);
  STAMP(2);

  float* st_m = (float*)(smem + L_STAT);
  float* st_r = st_m + 128;
  float* csum = (float*)(smem + L_CS);
  s1 += __shfl_xor(s1, 16); s1 += __shfl_xor(s1, 32);
  s2 += __shfl_xor(s2, 16); s2 += __shfl_xor(s2, 32);
  cs0 += __shfl_xor(cs0, 16); cs0 += __shfl_xor(cs0, 32);
  cs1 += __shfl_xor(cs1, 16); cs1 += __shfl_xor(cs1, 32);
  if (g == 0) {
    float m = s1 * (1.f / 640.f);
    float var = fmaxf(s2 * (1.f / 640.f) - m * m, 0.f);
    float r = __builtin_amdgcn_rsqf(var + 1e-5f);
    if (TYPE == 2) {
      float v2 = var * r * r;
      r *= __builtin_amdgcn_rsqf(v2 + 1e-5f);
    }
    int row = 32 * rg + 16 * hN + l15;
    st_m[row] = m; st_r[row] = r;
    csum[hN * 112 + rg * 16 + l15] = cs0;
    if (rg < 3) csum[hN * 112 + (rg + 4) * 16 + l15] = cs1;
  }
  asm volatile("s_waitcnt lgkmcnt(0)" ::: "memory");
  BARRIER();
  STAMP(3);

  if (TYPE == 0) {
    f32x4 mv[2], rv[2];
#pragma unroll
    for (int rt = 0; rt < 2; ++rt) {
      mv[rt] = *(const f32x4*)(st_m + 32 * rg + 16 * rt + 4 * g);
      rv[rt] = *(const f32x4*)(st_r + 32 * rg + 16 * rt + 4 * g);
    }
    s16x8 fr[5];
#pragma unroll
    for (int u = 0; u < 5; ++u) {
      float c = csum[hN * 112 + u * 16 + l15];
      unsigned pw[4];
#pragma unroll
      for (int rt = 0; rt < 2; ++rt) {
        float o[4];
#pragma unroll
        for (int i = 0; i < 4; ++i) {
          float v = (acc[rt][u][i] - mv[rt][i] * c) * rv[rt][i];
          if (u < 2) o[i] = phi_f(v);
          else if (u == 2) { float ph = phi_f(v); o[i] = (l15 < 8) ? ph : v; }
          else o[i] = v;
        }
        pw[2 * rt] = pk_bf16(o[0], o[1]); pw[2 * rt + 1] = pk_bf16(o[2], o[3]);
      }
      fr[u] = __builtin_bit_cast(s16x8, (u32x4){pw[0], pw[1], pw[2], pw[3]});
    }
    const s16x8 ones = __builtin_bit_cast(s16x8, (u32x4){0x3F803F80u, 0x3F803F80u, 0x3F803F80u, 0x3F803F80u});
    const f32x4 z4 = (f32x4){0.f, 0.f, 0.f, 0.f};
    char* sc = smem + hN * 2 * STG + rg * 10240 + lane * 16;
    *(f32x4*)(sc + 0 * 1024) = MFMA16(fr[0], fr[2], z4);
    *(f32x4*)(sc + 1 * 1024) = MFMA16(fr[0], fr[3], z4);
    *(f32x4*)(sc + 2 * 1024) = MFMA16(fr[1], fr[2], z4);
    *(f32x4*)(sc + 3 * 1024) = MFMA16(fr[1], fr[3], z4);
    *(f32x4*)(sc + 4 * 1024) = MFMA16(fr[1], fr[4], z4);
    *(f32x4*)(sc + 5 * 1024) = MFMA16(fr[2], fr[3], z4);
    *(f32x4*)(sc + 6 * 1024) = MFMA16(fr[2], fr[4], z4);
    *(f32x4*)(sc + 7 * 1024) = MFMA16(fr[0], ones, z4);
    *(f32x4*)(sc + 8 * 1024) = MFMA16(fr[1], ones, z4);
    *(f32x4*)(sc + 9 * 1024) = MFMA16(fr[2], ones, z4);
  }

  {
    char* act = ws + WS_ACT;
    float mS[2], sc[2];
#pragma unroll
    for (int rt = 0; rt < 2; ++rt) {
      int row = 32 * rg + 16 * rt + l15;
      sc[rt] = st_r[row];
      mS[rt] = -st_m[row] * sc[rt];
    }
    char* abase = act + (size_t)(rb * 8 + rg * 2) * KS_TOT * 1024 + lane * 16;
#pragma unroll
    for (int u = (TYPE == 0 ? 5 : 0); u < (TYPE == 1 ? 4 : 7); ++u) {
      const int U = U0 + u;
      f32x4 c = *(const f32x4*)(csum + hN * 112 + u * 16 + 4 * g);
      f32x4 bs = (f32x4){0.f, 0.f, 0.f, 0.f};
      if (TYPE == 2) bs = (f32x4){bf_lo(bias[u][0]), bf_hi(bias[u][0]), bf_lo(bias[u][1]), bf_hi(bias[u][1])};
#pragma unroll
      for (int rt = 0; rt < 2; ++rt) {
        float o[4];
#pragma unroll
        for (int i = 0; i < 4; ++i) {
          float v = __builtin_fmaf(acc[rt][u][i], sc[rt], __builtin_fmaf(mS[rt], c[i], bs[i]));
          o[i] = (TYPE == 2) ? gelu_f(v) : phi_f(v);
        }
        u32x2 pk = (u32x2){pk_bf16(o[0], o[1]), pk_bf16(o[2], o[3])};
        if (TYPE != 2 || U < 96)
          *(u32x2*)(abase + (size_t)(rt * KS_TOT + (U >> 1)) * 1024 + (U & 1) * 8) = pk;
      }
    }
  }
  STAMP(4);
  if (TYPE == 0) {
    asm volatile("s_waitcnt lgkmcnt(0)" ::: "memory");
    BARRIER();
    const int t = w * 64 + lane;
    if (t < 420) {
      int hh = t / 105, rem = t - hh * 105, e = rem / 5, d4 = rem - e * 5;
      int hl = hh & 1;
      int dk = 20 * hl + 4 * d4, ua = dk >> 4, gg = (dk & 15) >> 2;
      int tile, ln;
      if (e == 20) { tile = 7 + ua; ln = 0; }
      else {
        int ev = 40 + 20 * hl + e, ub = ev >> 4; ln = ev & 15;
        tile = (ua == 0) ? ub - 2 : (ua == 1) ? ub : ub + 2;
      }
      const char* src = smem + (hh >> 1) * 2 * STG + tile * 1024 + (gg * 16 + ln) * 16;
      f32x4 v = *(const f32x4*)src;
#pragma unroll
      for (int r = 1; r < 4; ++r) v += *(const f32x4*)(src + r * 10240);
      float* kvp = (float*)(ws + WS_KVP) + ((size_t)rb * 32 + 4 * ct) * 420;
      ((f32x4*)kvp)[t] = v;
    }
  }
  STAMP(5);
}

__global__ __launch_bounds__(640, 3) void k1(const char* __restrict__ x, const char* __restrict__ wqkv,
                                            const char* __restrict__ wo, const char* __restrict__ w1,
                                            const char* __restrict__ b1, const char* __restrict__ w2,
                                            char* __restrict__ ws) {
  __shared__ __attribute__((aligned(16))) char smem[K1_LDS];
  const int tid = threadIdx.x, lane = tid & 63;
  const int w = __builtin_amdgcn_readfirstlane(tid >> 6);
  const int bid = blockIdx.x;
  constexpr int KID = 0;
  STAMP(0);
  const int xcd = bid & 7, idx = bid >> 3;
  const int rb = (xcd & 1) * 8 + (idx & 7);
  const int ct = (xcd >> 1) * 4 + (idx >> 3);
  int kws = 0, hL = 0, ch0 = 0;
  if (ct < 8) { int kb = 1280 + 160 * ct, L = kb & ~127; kws = L + min(kb - L, 64); ch0 = (kb - kws) >> 4; }
  else if (ct > 8) { int hb = 448 * (ct - 9); hL = hb & ~127; ch0 = (hb - hL) >> 4; }

  if (w < 8) {
    if (ct < 8) k1_compute<0>(smem, b1, ws, w, lane, rb, ct, ch0);
    else if (ct == 8) k1_compute<1>(smem, b1, ws, w, lane, rb, ct, ch0);
    else k1_compute<2>(smem, b1, ws, w, lane, rb, ct, ch0);
    return;
  }
  const int ld = w - 8;
  const unsigned lbase = (unsigned)(uintptr_t)(LAS char*)smem;
  const char* wsrc = (ct < 9) ? wqkv : w1;
  const unsigned ldb = (ct < 9) ? 3840u : 3072u;
  unsigned aoff[2], boff[4];
#pragma unroll
  for (int i = 0; i < 2; ++i) {
    int f = (4 * i + (lane >> 4)) & 7;
    aoff[i] = (unsigned)((64 * ld + (lane >> 3)) * 1280 + (((lane & 7) ^ f) << 4));
  }
#pragma unroll
  for (int v = 0; v < 4; ++v) {
    int s = ((2 * (v & 1) + (lane >> 5)) & 3) | ((v >> 1) << 2);
    int ch = (lane & 31) ^ (s << 1);
    int cb;
    if (ct < 8) cb = ch < 12 ? kws + 16 * ch : (ch < 24 ? kws + 1280 + 16 * (ch - 12) : 128 * ct + 16 * (ch - 24));
    else if (ct == 8) cb = 1024 + 16 * (ch & 15);
    else { cb = hL + 16 * ch; if (cb >= 3072) cb -= 128; }
    boff[v] = (unsigned)((32 * ld + (lane >> 5)) * ldb + cb);
  }
  const char* xsrc = x + (size_t)rb * 128 * 1280;
  int kcur = ((idx & 7) + 3 * (idx >> 3) + 5 * (xcd & 1)) % 10;
  auto stage = [&](unsigned sb) {
    const char* xs = xsrc + kcur * 128;
    const char* wsb = wsrc + (size_t)kcur * 64 * ldb;
#pragma unroll
    for (int i = 0; i < 8; ++i) glds_s(xs + i * 8 * 1280, aoff[i & 1], lbase + sb + (64 * ld + 8 * i) * 128);
#pragma unroll
    for (int i = 0; i < 16; ++i)
      glds_s(wsb + (size_t)(2 * i) * ldb, boff[(i & 1) | (((i >> 2) & 1) << 1)], lbase + sb + 16384 + (32 * ld + 2 * i) * 512);
    kcur = (kcur == 9) ? 0 : kcur + 1;
  };
  stage(0);
  stage(STG);
  if (bid < 16) ((u32x4*)(ws + WS_KVC))[bid * 128 + ld * 64 + lane] = (u32x4){SENT, SENT, SENT, SENT};
  unsigned nb = 2 * STG;
#pragma unroll 1
  for (int kt = 0; kt < 8; ++kt) {
    WAIT_VM(24);
    BARRIER();
    stage(nb);
    nb += STG; if (nb >= 3 * STG) nb = 0;
  }
  WAIT_VM(24);
  BARRIER();
  const int p0 = bid * 2 + ld;
  const int np = (p0 + 512 < 680) ? 2 : 1;
  for (int j = 0; j < np; ++j) {
    const int piece = p0 + 512 * j, pks = piece / 10, pcg = piece - pks * 10;
    const char* src = ((pks < 48) ? (w2 + (size_t)pks * 32 * 1280) : (wo + (size_t)(pks - 48) * 32 * 1280)) + pcg * 128;
#pragma unroll
    for (int i = 0; i < 4; ++i)
      glds_s(src + i * 8 * 1280, (unsigned)((lane >> 3) * 1280 + (lane & 7) * 16), lbase + STG + (ld * 2 + j) * 4096 + i * 1024);
  }
  if (np == 2) { WAIT_VM(8); } else { WAIT_VM(4); }
  BARRIER();
  BARRIER();
  WAIT_VM(0);
  {
    const int g = lane >> 4, q4 = (lane & 15) >> 2, p4 = lane & 3;
    for (int j = 0; j < np; ++j) {
      const int piece = p0 + 512 * j, pks = piece / 10, pcg = piece - pks * 10;
      char* dst = ws + WS_WT + (size_t)pks * 1024 + lane * 16;
      unsigned base = (unsigned)(STG + (ld * 2 + j) * 4096 + (4 * g + q4) * 128 + p4 * 8);
#pragma unroll
      for (int n = 0; n < 4; ++n) {
        s16x8 f = tr_frag(smem, base + n * 32, base + n * 32 + 2048);
        *(s16x8*)(dst + (size_t)(pcg * 4 + n) * KS_TOT * 1024) = f;
      }
    }
  }
  if (ct < 8) BARRIER();
}

constexpr int RING = 3;
constexpr int SLOT = 9216;
constexpr int K2_KV = 4 * RING * SLOT;
constexpr int K2_ZERO = K2_KV + 4 * 6784;
constexpr int K2_LDS = K2_ZERO + 16;

__global__ __launch_bounds__(512, 2) void k2(const char* __restrict__ x, const char* __restrict__ b2,
                                            char* __restrict__ ws, char* __restrict__ out) {
  __shared__ __attribute__((aligned(16))) char smem[K2_LDS];
  const int tid = threadIdx.x, lane = tid & 63;
  const int wv = __builtin_amdgcn_readfirstlane(tid >> 6);
  const int w = wv & 3;
  const int bid = blockIdx.x;
  const int xcd = bid & 7, idx = bid >> 3;
  const int rtl = (xcd >> 1) * 8 + (idx & 7);
  const int cl = (xcd & 1) * 4 + (idx >> 3);
  const int g = lane >> 4, l15 = lane & 15;
  constexpr int KID = 1;
  STAMP(0);
  const char* act = ws + WS_ACT + (size_t)rtl * 4 * KS_TOT * 1024;
  const char* wt = ws + WS_WT + (size_t)cl * 5 * KS_TOT * 1024;
  const int ks0 = 12 * w, kso = 48 + 5 * w;

  if (wv >= 4) {
    const unsigned lb = (unsigned)(uintptr_t)(LAS char*)smem + w * RING * SLOT;
    const unsigned voff = lane * 16;
    auto issue = [&](int s) {
      const unsigned d = lb + (s % RING) * SLOT;
      const int ks = (s < 12) ? ks0 + s : kso + (s - 12);
#pragma unroll
      for (int n = 0; n < 5; ++n) glds_s(wt + ((size_t)n * KS_TOT + ks) * 1024, voff, d + n * 1024);
      if (s < 12) {
#pragma unroll
        for (int r = 0; r < 4; ++r) glds_s(act + ((size_t)r * KS_TOT + ks) * 1024, voff, d + (5 + r) * 1024);
      }
    };
    const int rhead = bid >> 3, rdw = (bid & 7) * 27 + min(lane, 26);
    if (w == 0) {
      float2 part[16];
      const float* src = (const float*)(ws + WS_KVP) + (size_t)rhead * 420 + min(rdw, 209) * 2;
#pragma unroll
      for (int p = 0; p < 16; ++p) part[p] = *(const float2*)(src + (size_t)p * 32 * 420);
      __builtin_amdgcn_sched_barrier(0);
      issue(0);
      issue(1);
      __builtin_amdgcn_sched_barrier(0);
      float a = 0.f, b = 0.f;
#pragma unroll
      for (int p = 0; p < 16; ++p) { a += part[p].x; b += part[p].y; }
      unsigned v = pk_bf16(a, b);
      if (v == SENT) v = 0x7FC07FC0u;
      if (lane < 27 && rdw < 210)
        __hip_atomic_store((GAS unsigned*)(ws + WS_KVC) + rhead * 256 + rdw, v, __ATOMIC_RELAXED, __HIP_MEMORY_SCOPE_AGENT);
    } else {
      issue(0);
      issue(1);
    }
    if (lane < 4) *(unsigned*)(smem + K2_ZERO + lane * 4) = 0u;
    const char* kvb = smem + K2_KV + w * 6784;
    const f32x4 z4 = (f32x4){0.f, 0.f, 0.f, 0.f};
    const __amdgpu_buffer_rsrc_t rs = __builtin_amdgcn_make_buffer_rsrc((void*)(ws + WS_KVC), 0, 32768, 0x00020000);
    s16x8 pq[4][5];
    u32x4 kvv[8];
    f32x4 den[2][4];
    s16x8 of[5][4];
    auto kvfrag = [&](int he, int ebase, int b) -> s16x8 {
      u32x2 h2[2];
#pragma unroll
      for (int hh = 0; hh < 2; ++hh) {
        int d = 32 * b + 16 * hh + 4 * g;
        int hd = d / 20;
        const char* p = (hd == he) ? (kvb + ebase + (d - 20 * hd) * 2) : (smem + K2_ZERO);
        h2[hh] = *(const u32x2*)p;
      }
      return __builtin_bit_cast(s16x8, (u32x4){h2[0][0], h2[0][1], h2[1][0], h2[1][1]});
    };
    auto step = [&](auto sc) {
      constexpr int s = decltype(sc)::value;
      asm volatile("s_waitcnt lgkmcnt(0)" ::: "memory");
      if constexpr (s + 1 < 12) { WAIT_VM(9); } else if constexpr (s + 1 < 17) { WAIT_VM(5); } else { WAIT_VM(0); }
      BARRIER();
      if constexpr (s + 2 < 17) issue(s + 2);
      __builtin_amdgcn_sched_barrier(0);
      if constexpr (s >= 10 && s < 15) {
#pragma unroll
        for (int r = 0; r < 4; ++r)
          *(LAS s16x8*)(smem + w * RING * SLOT + ((s + 2) % RING) * SLOT + (5 + r) * 1024 + lane * 16) = of[s - 10][r];
      }
      if constexpr (s == 1 || s == 2) {
#pragma unroll
        for (int bb = 0; bb < 5; ++bb)
#pragma unroll
          for (int r = 2 * (s - 1); r < 2 * s; ++r) pq[r][bb] = *(const s16x8*)(act + ((size_t)r * KS_TOT + kso + bb) * 1024 + lane * 16);
      }
      if constexpr (s == 3) {
#pragma unroll
        for (int h = 0; h < 8; ++h) kvv[h] = __builtin_amdgcn_raw_buffer_load_b128(rs, (unsigned)((8 * w + h) * 1024 + lane * 16), 0, 16);
      }
      if constexpr (s == 4) {
        for (int spin = 0; spin < (1 << 20); ++spin) {
          bool bad = false;
#pragma unroll
          for (int h = 0; h < 8; ++h) {
            bool b01 = (kvv[h][0] == SENT) || (kvv[h][1] == SENT);
            bool b23 = (kvv[h][2] == SENT) || (kvv[h][3] == SENT);
            bad |= (lane < 52) ? (b01 || b23) : ((lane == 52) ? b01 : false);
          }
          if (!__any(bad)) break;
          __builtin_amdgcn_s_sleep(2);
#pragma unroll
          for (int h = 0; h < 8; ++h) kvv[h] = __builtin_amdgcn_raw_buffer_load_b128(rs, (unsigned)((8 * w + h) * 1024 + lane * 16), 0, 16);
        }
        if (lane < 53) {
#pragma unroll
          for (int h = 0; h < 8; ++h) *(u32x4*)(smem + K2_KV + w * 6784 + h * 848 + lane * 16) = kvv[h];
        }
      }
      if constexpr (s == 4 || s == 5) {
        const int c = s - 4;
        int he = 4 * c + (l15 & 3);
        int eb = he * 848 + 800;
#pragma unroll
        for (int r = 0; r < 4; ++r) den[c][r] = z4;
#pragma unroll
        for (int bb = 0; bb < 3; ++bb) {
          s16x8 zf = kvfrag(he, eb, 2 * c + bb);
#pragma unroll
          for (int r = 0; r < 4; ++r) den[c][r] = MFMA16(zf, pq[r][2 * c + bb], den[c][r]);
        }
#pragma unroll
        for (int r = 0; r < 4; ++r)
#pragma unroll
          for (int i = 0; i < 4; ++i) den[c][r][i] = __builtin_amdgcn_rcpf(den[c][r][i] + 1e-6f);
      }
      if constexpr (s >= 6 && s < 11) {
        const int bp = s - 6;
        unsigned ow[4][4];
#pragma unroll
        for (int hf = 0; hf < 2; ++hf) {
          const int jt = 2 * bp + hf;
          int e = 16 * jt + l15, he = e / 20, eb = he * 848 + (e - 20 * he) * 40;
          const int dlo = 20 * ((16 * jt) / 20), dhi = 20 * ((16 * jt + 15) / 20) + 19;
          const int blo = dlo / 32, bhi = dhi / 32;
          f32x4 num[4];
#pragma unroll
          for (int r = 0; r < 4; ++r) num[r] = z4;
#pragma unroll
          for (int b = 0; b < 5; ++b) {
            if (b < blo || b > bhi) continue;
            s16x8 kf = kvfrag(he, eb, b);
#pragma unroll
            for (int r = 0; r < 4; ++r) num[r] = MFMA16(kf, pq[r][b], num[r]);
          }
          const int h0 = (4 * jt) / 5, h1 = (4 * jt + 3) / 5;
          const bool hi = g >= 5 * h1 - 4 * jt;
#pragma unroll
          for (int r = 0; r < 4; ++r) {
            float rd = den[h0 >> 2][r][h0 & 3];
            if (h1 != h0) rd = hi ? den[h1 >> 2][r][h1 & 3] : rd;
            ow[r][2 * hf] = pk_bf16(num[r][0] * rd, num[r][1] * rd);
            ow[r][2 * hf + 1] = pk_bf16(num[r][2] * rd, num[r][3] * rd);
          }
        }
#pragma unroll
        for (int r = 0; r < 4; ++r) of[bp][r] = __builtin_bit_cast(s16x8, (u32x4){ow[r][0], ow[r][1], ow[r][2], ow[r][3]});
      }
    };
#define STEP(i) step(IC<i>{})
    STEP(0); STEP(1); STEP(2); STEP(3); STEP(4); STEP(5); STEP(6); STEP(7); STEP(8);
    STEP(9); STEP(10); STEP(11); STEP(12); STEP(13); STEP(14); STEP(15); STEP(16);
#undef STEP
    BARRIER();
    BARRIER();
    return;
  }

  const int row = rtl * 64 + w * 16 + l15;
  u32x2 xv[5], bv[5];
#pragma unroll
  for (int n = 0; n < 5; ++n) {
    const int col = cl * 80 + n * 16 + 4 * g;
    xv[n] = *(const u32x2*)(x + ((size_t)row * 640 + col) * 2);
    bv[n] = *(const u32x2*)(b2 + col * 2);
  }
  f32x4 acc[5][4];
#pragma unroll
  for (int n = 0; n < 5; ++n)
#pragma unroll
    for (int r = 0; r < 4; ++r) acc[n][r] = (f32x4){0.f, 0.f, 0.f, 0.f};
  const char* ring = smem + w * RING * SLOT + lane * 16;
  STAMP(1);
  unsigned so = 0;
#pragma unroll 1
  for (int s = 0; s < 17; ++s) {
    BARRIER();
    const char* sl = ring + so;
    s16x8 cb[5], ca[4];
#pragma unroll
    for (int r = 0; r < 4; ++r) ca[r] = *(const LAS s16x8*)(sl + (5 + r) * 1024);
#pragma unroll
    for (int n = 0; n < 5; ++n) cb[n] = *(const LAS s16x8*)(sl + n * 1024);
#pragma unroll
    for (int n = 0; n < 5; ++n)
#pragma unroll
      for (int r = 0; r < 4; ++r) acc[n][r] = MFMA16(cb[n], ca[r], acc[n][r]);
    so += SLOT; if (so >= RING * SLOT) so = 0;
  }
  STAMP(2);
  BARRIER();

  {
    char* red = smem;
#pragma unroll
    for (int r = 0; r < 4; ++r) {
      if (r == w) continue;
      int slot = (w < r) ? w : w - 1;
#pragma unroll
      for (int n = 0; n < 5; ++n) *(f32x4*)(red + ((r * 3 + slot) * 5 + n) * 1024 + lane * 16) = acc[n][r];
    }
    asm volatile("s_waitcnt lgkmcnt(0)" ::: "memory");
    BARRIER();
    STAMP(3);
#pragma unroll
    for (int n = 0; n < 5; ++n) {
      f32x4 v = (w == 0) ? acc[n][0] : (w == 1) ? acc[n][1] : (w == 2) ? acc[n][2] : acc[n][3];
#pragma unroll
      for (int sidx = 0; sidx < 3; ++sidx) v += *(const f32x4*)(red + ((w * 3 + sidx) * 5 + n) * 1024 + lane * 16);
      const int col = cl * 80 + n * 16 + 4 * g;
      float o0 = v[0] + bf_lo(xv[n][0]) + bf_lo(bv[n][0]);
      float o1 = v[1] + bf_hi(xv[n][0]) + bf_hi(bv[n][0]);
      float o2 = v[2] + bf_lo(xv[n][1]) + bf_lo(bv[n][1]);
      float o3 = v[3] + bf_hi(xv[n][1]) + bf_hi(bv[n][1]);
      *(u32x2*)(out + ((size_t)row * 640 + col) * 2) = (u32x2){pk_bf16(o0, o1), pk_bf16(o2, o3)};
    }
  }
  STAMP(4);
}

}

extern "C" void kernel_launch(void* const* d_in, const int* in_sizes, int n_in,
                              void* d_out, int out_size, void* d_ws, size_t ws_size,
                              hipStream_t stream) {
  const char* x = (const char*)d_in[0];
  const char* wqkv = (const char*)d_in[1];
  const char* wo = (const char*)d_in[2];
  const char* w1 = (const char*)d_in[4];
  const char* b1 = (const char*)d_in[5];
  const char* w2 = (const char*)d_in[6];
  const char* b2 = (const char*)d_in[7];
  k1<<<256, 640, 0, stream>>>(x, wqkv, wo, w1, b1, w2, (char*)d_ws);
  k2<<<256, 512, 0, stream>>>(x, b2, (char*)d_ws, (char*)d_out);
}
```

```cpp
#include <hip/hip_runtime.h>
#include <hip/hip_bf16.h>
#include <stdint.h>

typedef short s16x8 __attribute__((ext_vector_type(8)));
typedef short s16x4 __attribute__((ext_vector_type(4)));
typedef float f32x4 __attribute__((ext_vector_type(4)));
typedef unsigned u32x4 __attribute__((ext_vector_type(4)));
typedef unsigned u32x2 __attribute__((ext_vector_type(2)));
typedef __bf16 bf16x2v __attribute__((ext_vector_type(2)));
#define LAS __attribute__((address_space(3)))
#define GAS __attribute__((address_space(1)))
#define DI __device__ __forceinline__

namespace {

template <int V> struct IC { static constexpr int value = V; };

constexpr int KS_TOT = 68;
constexpr size_t ACT_BYTES = (size_t)128 * KS_TOT * 1024;
constexpr size_t WT_BYTES = (size_t)40 * KS_TOT * 1024;
constexpr size_t KVP_BYTES = (size_t)16 * 32 * 420 * 4;
constexpr size_t WS_ACT = 0;
constexpr size_t WS_WT = WS_ACT + ACT_BYTES;
constexpr size_t WS_KVP = WS_WT + WT_BYTES;
constexpr size_t WS_KVC = WS_KVP + KVP_BYTES;
constexpr unsigned SENT = 0xFFFFFFFFu;

constexpr int STG = 49152;
constexpr int L_STAT = 3 * STG;
constexpr int L_CS = L_STAT + 1536;
constexpr int L_BIAS = L_CS + 896;
constexpr int K1_LDS = L_BIAS + 1024;

DI unsigned pk_bf16(float a, float b) {
  bf16x2v v; v[0] = (__bf16)a; v[1] = (__bf16)b;
  return __builtin_bit_cast(unsigned, v);
}
DI float bf_lo(unsigned u) { return __uint_as_float(u << 16); }
DI float bf_hi(unsigned u) { return __uint_as_float(u & 0xffff0000u); }
DI float dot2(unsigned a, unsigned b, float c) {
  return __builtin_amdgcn_fdot2_f32_bf16(__builtin_bit_cast(bf16x2v, a), __builtin_bit_cast(bf16x2v, b), c, false);
}
DI float phi_f(float t) {
  float e = __builtin_amdgcn_exp2f(t * 1.4426950408889634f);
  return (t > 0.f ? t + 1.f : e) + 1e-6f;
}
DI float gelu_f(float v) {
  const float c1 = -2.f * 0.7978845608028654f * 1.4426950408889634f;
  const float c2 = c1 * 0.044715f;
  float p = __builtin_fmaf(v * v, c2, c1);
  float e = __builtin_amdgcn_exp2f(v * p);
  return v * __builtin_amdgcn_rcpf(1.f + e);
}
DI s16x8 tr_frag(const char* smem, unsigned off0, unsigned off1) {
  s16x4 lo = __builtin_amdgcn_ds_read_tr16_b64_v4i16((LAS s16x4*)(smem + off0));
  s16x4 hi = __builtin_amdgcn_ds_read_tr16_b64_v4i16((LAS s16x4*)(smem + off1));
  return __builtin_shufflevector(lo, hi, 0, 1, 2, 3, 4, 5, 6, 7);
}
#define MFMA16(a, b, c) __builtin_amdgcn_mfma_f32_16x16x32_bf16((a), (b), (c), 0, 0, 0)
DI void glds_s(const char* sbase, unsigned voff, unsigned ldst) {
  unsigned keep;
  asm volatile("s_mov_b32 %0, m0\n\ts_mov_b32 m0, %3\n\ts_nop 0\n\tglobal_load_lds_dwordx4 %1, %2\n\ts_mov_b32 m0, %0"
               : "=&s"(keep) : "v"(voff), "s"(sbase), "s"(ldst) : "memory");
}
#define WAIT_VM(n) asm volatile("s_waitcnt vmcnt(" #n ")" ::: "memory")
#define BARRIER() do { asm volatile("" ::: "memory"); __builtin_amdgcn_s_barrier(); asm volatile("" ::: "memory"); } while (0)
#define STAMP(i)

template <int RG, bool KV>
DI void k1_loop(const char* smem, unsigned (&ard)[2], unsigned (&brd)[7], int hN,
                f32x4 (&acc)[2][7], float& s1, float& s2, float& cs0, float& cs1) {
  const unsigned ONES = 0x3F803F80u;
  s16x8 a0[2], a1[2], bx[4], by[3];
  auto rdA = [&](s16x8 (&a)[2], int s) {
    a[0] = *(const LAS s16x8*)(smem + ard[s]);
    a[1] = *(const LAS s16x8*)(smem + ard[s] + 2048);
  };
  auto rdX = [&](int s) {
#pragma unroll
    for (int u = 0; u < 4; ++u) bx[u] = tr_frag(smem, brd[u] + s * 16384, brd[u] + s * 16384 + 2048);
  };
  auto rdY = [&](int s) {
#pragma unroll
    for (int u = 4; u < 7; ++u) by[u - 4] = tr_frag(smem, brd[u] + s * 16384, brd[u] + s * 16384 + 2048);
  };
  auto mm1 = [&](const s16x8 (&a)[2], const s16x8& b, int u) {
    if (KV && u < 5) {
      acc[0][u] = MFMA16(a[0], b, acc[0][u]);
      acc[1][u] = MFMA16(a[1], b, acc[1][u]);
    } else {
      acc[0][u] = MFMA16(b, a[0], acc[0][u]);
      acc[1][u] = MFMA16(b, a[1], acc[1][u]);
    }
  };
  auto mmX = [&](const s16x8 (&a)[2]) {
#pragma unroll
    for (int u = 0; u < 4; ++u) mm1(a, bx[u], u);
    u32x4 bv = __builtin_bit_cast(u32x4, bx[RG]);
#pragma unroll
    for (int j = 0; j < 4; ++j) cs0 = dot2(bv[j], ONES, cs0);
    u32x4 av = __builtin_bit_cast(u32x4, hN ? a[1] : a[0]);
#pragma unroll
    for (int j = 0; j < 4; ++j) { s1 = dot2(av[j], ONES, s1); s2 = dot2(av[j], av[j], s2); }
  };
  auto mmY = [&](const s16x8 (&a)[2]) {
#pragma unroll
    for (int u = 4; u < 7; ++u) mm1(a, by[u - 4], u);
    if (RG < 3) {
      u32x4 bw = __builtin_bit_cast(u32x4, by[RG < 3 ? RG : 0]);
#pragma unroll
      for (int j = 0; j < 4; ++j) cs1 = dot2(bw[j], ONES, cs1);
    }
  };
#define SBK() __builtin_amdgcn_sched_barrier(0)
  unsigned sb = 0;
  BARRIER();
  rdA(a0, 0); rdX(0);
#pragma unroll 1
  for (int kt = 0; kt < 10; ++kt) {
    rdY(0); SBK(); mmX(a0); SBK();
    rdA(a1, 1); rdX(1); SBK(); mmY(a0); SBK();
    rdY(1); SBK(); mmX(a1);
    sb += STG;
    unsigned dl = STG;
    if (sb >= 3 * STG) { sb = 0; dl = (unsigned)(-2 * STG); }
    ard[0] += dl; ard[1] += dl;
#pragma unroll
    for (int u = 0; u < 7; ++u) brd[u] += dl;
    SBK();
    if (kt < 9) {
      asm volatile("s_waitcnt lgkmcnt(0)" ::: "memory");
      BARRIER();
      rdA(a0, 0); rdX(0);
    }
    SBK(); mmY(a1); SBK();
  }
}

template <int TYPE>
DI void k1_compute(char* smem, const char* __restrict__ b1, char* __restrict__ ws, int w, int lane, int rb, int ct, int ch0) {
  constexpr int KID = 0;
  const int rg = w & 3, hN = w >> 2;
  unsigned ard[2], brd[7];
  {
    const int g = lane >> 4, l15 = lane & 15, q4 = l15 >> 2, p4 = lane & 3;
    int row = 32 * rg + l15, f = (row >> 1) & 7;
    ard[0] = (unsigned)(row * 128 + ((g ^ f) << 4));
    ard[1] = ard[0] ^ 64u;
    int sx = q4 | ((g & 1) << 2);
#pragma unroll
    for (int u = 0; u < 7; ++u) {
      int xx = 16 * u + 4 * p4, c4;
      if (TYPE == 0) {
        int ck = 2 * ch0 + 10 * hN + 4 * u + p4;
        int cv = 2 * (12 + ch0) + 10 * hN + 4 * u + p4 - 10;
        int cq = 48 + 8 * hN + 4 * u + p4 - 20;
        c4 = (u < 2) ? ck : (u == 2) ? (p4 < 2 ? ck : cv) : (u < 5) ? cv : cq;
      } else if (TYPE == 1) c4 = ((64 * hN + xx) / 4) & 31;
      else c4 = (2 * ch0 + (112 * hN + xx) / 4) & 63;
      int ch = c4 >> 1, hf = c4 & 1;
      brd[u] = (unsigned)(16384 + (8 * g + q4) * 512 + ((ch ^ (sx << 1)) << 4) + hf * 8);
    }
  }
  const int U0 = (TYPE == 0) ? 96 + 4 * ct + 2 * hN - 5 : (TYPE == 1) ? 128 + 4 * hN : 14 * (ct - 9) + 7 * hN;

  f32x4 acc[2][7];
#pragma unroll
  for (int i = 0; i < 2; ++i)
#pragma unroll
    for (int u = 0; u < 7; ++u) acc[i][u] = (f32x4){0.f, 0.f, 0.f, 0.f};
  float s1 = 0.f, s2 = 0.f, cs0 = 0.f, cs1 = 0.f;
  STAMP(1);
  if (rg == 0) k1_loop<0, TYPE == 0>(smem, ard, brd, hN, acc, s1, s2, cs0, cs1);
  else if (rg == 1) k1_loop<1, TYPE == 0>(smem, ard, brd, hN, acc, s1, s2, cs0, cs1);
  else if (rg == 2) k1_loop<2, TYPE == 0>(smem, ard, brd, hN, acc, s1, s2, cs0, cs1);
  else k1_loop<3, TYPE == 0>(smem, ard, brd, hN, acc, s1, s2, cs0, cs1);
  STAMP(2);
  lane = __builtin_amdgcn_mbcnt_hi(~0u, __builtin_amdgcn_mbcnt_lo(~0u, 0u));
  asm volatile("" : "+v"(lane));
  const int g = lane >> 4, l15 = lane & 15;

  float* st_m = (float*)(smem + L_STAT);
  float* st_r = st_m + 128;
  float* csum = (float*)(smem + L_CS);
  s1 += __shfl_xor(s1, 16); s1 += __shfl_xor(s1, 32);
  s2 += __shfl_xor(s2, 16); s2 += __shfl_xor(s2, 32);
  cs0 += __shfl_xor(cs0, 16); cs0 += __shfl_xor(cs0, 32);
  cs1 += __shfl_xor(cs1, 16); cs1 += __shfl_xor(cs1, 32);
  if (g == 0) {
    float m = s1 * (1.f / 640.f);
    float var = fmaxf(s2 * (1.f / 640.f) - m * m, 0.f);
    float r = __builtin_amdgcn_rsqf(var + 1e-5f);
    if (TYPE == 2) {
      float v2 = var * r * r;
      r *= __builtin_amdgcn_rsqf(v2 + 1e-5f);
    }
    int row = 32 * rg + 16 * hN + l15;
    st_m[row] = m; st_r[row] = r;
    csum[hN * 112 + rg * 16 + l15] = cs0;
    if (rg < 3) csum[hN * 112 + (rg + 4) * 16 + l15] = cs1;
  }
  asm volatile("s_waitcnt lgkmcnt(0)" ::: "memory");
  BARRIER();
  STAMP(3);

  if (TYPE == 0) {
    f32x4 mv[2], rv[2];
#pragma unroll
    for (int rt = 0; rt < 2; ++rt) {
      mv[rt] = *(const f32x4*)(st_m + 32 * rg + 16 * rt + 4 * g);
      rv[rt] = *(const f32x4*)(st_r + 32 * rg + 16 * rt + 4 * g);
    }
    s16x8 fr[5];
#pragma unroll
    for (int u = 0; u < 5; ++u) {
      float c = csum[hN * 112 + u * 16 + l15];
      unsigned pw[4];
#pragma unroll
      for (int rt = 0; rt < 2; ++rt) {
        float o[4];
#pragma unroll
        for (int i = 0; i < 4; ++i) {
          float v = (acc[rt][u][i] - mv[rt][i] * c) * rv[rt][i];
          if (u < 2) o[i] = phi_f(v);
          else if (u == 2) { float ph = phi_f(v); o[i] = (l15 < 8) ? ph : v; }
          else o[i] = v;
        }
        pw[2 * rt] = pk_bf16(o[0], o[1]); pw[2 * rt + 1] = pk_bf16(o[2], o[3]);
      }
      fr[u] = __builtin_bit_cast(s16x8, (u32x4){pw[0], pw[1], pw[2], pw[3]});
    }
    const s16x8 ones = __builtin_bit_cast(s16x8, (u32x4){0x3F803F80u, 0x3F803F80u, 0x3F803F80u, 0x3F803F80u});
    const f32x4 z4 = (f32x4){0.f, 0.f, 0.f, 0.f};
    char* sc = smem + hN * 2 * STG + rg * 10240 + lane * 16;
    *(f32x4*)(sc + 0 * 1024) = MFMA16(fr[0], fr[2], z4);
    *(f32x4*)(sc + 1 * 1024) = MFMA16(fr[0], fr[3], z4);
    *(f32x4*)(sc + 2 * 1024) = MFMA16(fr[1], fr[2], z4);
    *(f32x4*)(sc + 3 * 1024) = MFMA16(fr[1], fr[3], z4);
    *(f32x4*)(sc + 4 * 1024) = MFMA16(fr[1], fr[4], z4);
    *(f32x4*)(sc + 5 * 1024) = MFMA16(fr[2], fr[3], z4);
    *(f32x4*)(sc + 6 * 1024) = MFMA16(fr[2], fr[4], z4);
    *(f32x4*)(sc + 7 * 1024) = MFMA16(fr[0], ones, z4);
    *(f32x4*)(sc + 8 * 1024) = MFMA16(fr[1], ones, z4);
    *(f32x4*)(sc + 9 * 1024) = MFMA16(fr[2], ones, z4);
  }

  {
    char* act = ws + WS_ACT;
    float mS[2], sc[2];
#pragma unroll
    for (int rt = 0; rt < 2; ++rt) {
      int row = 32 * rg + 16 * rt + l15;
      sc[rt] = st_r[row];
      mS[rt] = -st_m[row] * sc[rt];
    }
    char* abase = act + (size_t)(rb * 8 + rg * 2) * KS_TOT * 1024 + lane * 16;
#pragma unroll
    for (int u = (TYPE == 0 ? 5 : 0); u < (TYPE == 1 ? 4 : 7); ++u) {
      const int U = U0 + u;
      f32x4 c = *(const f32x4*)(csum + hN * 112 + u * 16 + 4 * g);
      f32x4 bs = (f32x4){0.f, 0.f, 0.f, 0.f};
      if (TYPE == 2) {
        u32x2 bb = *(const u32x2*)(smem + L_BIAS + (hN * 112 + u * 16 + 4 * g) * 2);
        bs = (f32x4){bf_lo(bb[0]), bf_hi(bb[0]), bf_lo(bb[1]), bf_hi(bb[1])};
      }
#pragma unroll
      for (int rt = 0; rt < 2; ++rt) {
        float o[4];
#pragma unroll
        for (int i = 0; i < 4; ++i) {
          float v = __builtin_fmaf(acc[rt][u][i], sc[rt], __builtin_fmaf(mS[rt], c[i], bs[i]));
          o[i] = (TYPE == 2) ? gelu_f(v) : phi_f(v);
        }
        u32x2 pk = (u32x2){pk_bf16(o[0], o[1]), pk_bf16(o[2], o[3])};
        if (TYPE != 2 || U < 96)
          *(u32x2*)(abase + (size_t)(rt * KS_TOT + (U >> 1)) * 1024 + (U & 1) * 8) = pk;
      }
    }
  }
  STAMP(4);
  if (TYPE == 0) {
    asm volatile("s_waitcnt lgkmcnt(0)" ::: "memory");
    BARRIER();
    const int t = w * 64 + lane;
    if (t < 420) {
      int hh = t / 105, rem = t - hh * 105, e = rem / 5, d4 = rem - e * 5;
      int hl = hh & 1;
      int dk = 20 * hl + 4 * d4, ua = dk >> 4, gg = (dk & 15) >> 2;
      int tile, ln;
      if (e == 20) { tile = 7 + ua; ln = 0; }
      else {
        int ev = 40 + 20 * hl + e, ub = ev >> 4; ln = ev & 15;
        tile = (ua == 0) ? ub - 2 : (ua == 1) ? ub : ub + 2;
      }
      const char* src = smem + (hh >> 1) * 2 * STG + tile * 1024 + (gg * 16 + ln) * 16;
      f32x4 v = *(const f32x4*)src;
#pragma unroll
      for (int r = 1; r < 4; ++r) v += *(const f32x4*)(src + r * 10240);
      float* kvp = (float*)(ws + WS_KVP) + ((size_t)rb * 32 + 4 * ct) * 420;
      ((f32x4*)kvp)[t] = v;
    }
  }
  STAMP(5);
}

__global__ __launch_bounds__(640, 3) void k1(const char* __restrict__ x, const char* __restrict__ wqkv,
                                            const char* __restrict__ wo, const char* __restrict__ w1,
                                            const char* __restrict__ b1, const char* __restrict__ w2,
                                            char* __restrict__ ws) {
  __shared__ __attribute__((aligned(16))) char smem[K1_LDS];
  const int tid = threadIdx.x, lane = tid & 63;
  const int w = __builtin_amdgcn_readfirstlane(tid >> 6);
  const int bid = blockIdx.x;
  constexpr int KID = 0;
  STAMP(0);
  const int xcd = bid & 7, idx = bid >> 3;
  const int rb = (xcd & 1) * 8 + (idx & 7);
  const int ct = (xcd >> 1) * 4 + (idx >> 3);
  int kws = 0, hL = 0, ch0 = 0;
  if (ct < 8) { int kb = 1280 + 160 * ct, L = kb & ~127; kws = L + min(kb - L, 64); ch0 = (kb - kws) >> 4; }
  else if (ct > 8) { int hb = 448 * (ct - 9); hL = hb & ~127; ch0 = (hb - hL) >> 4; }

  if (w < 8) {
    if (ct < 8) k1_compute<0>(smem, b1, ws, w, lane, rb, ct, ch0);
    else if (ct == 8) k1_compute<1>(smem, b1, ws, w, lane, rb, ct, ch0);
    else k1_compute<2>(smem, b1, ws, w, lane, rb, ct, ch0);
    return;
  }
  const int ld = w - 8;
  const unsigned lbase = (unsigned)(uintptr_t)(LAS char*)smem;
  const char* wsrc = (ct < 9) ? wqkv : w1;
  const unsigned ldb = (ct < 9) ? 3840u : 3072u;
  unsigned aoff[2], boff[4];
#pragma unroll
  for (int i = 0; i < 2; ++i) {
    int f = (4 * i + (lane >> 4)) & 7;
    aoff[i] = (unsigned)((64 * ld + (lane >> 3)) * 1280 + (((lane & 7) ^ f) << 4));
  }
#pragma unroll
  for (int v = 0; v < 4; ++v) {
    int s = ((2 * (v & 1) + (lane >> 5)) & 3) | ((v >> 1) << 2);
    int ch = (lane & 31) ^ (s << 1);
    int cb;
    if (ct < 8) cb = ch < 12 ? kws + 16 * ch : (ch < 24 ? kws + 1280 + 16 * (ch - 12) : 128 * ct + 16 * (ch - 24));
    else if (ct == 8) cb = 1024 + 16 * (ch & 15);
    else { cb = hL + 16 * ch; if (cb >= 3072) cb -= 128; }
    boff[v] = (unsigned)((32 * ld + (lane >> 5)) * ldb + cb);
  }
  const char* xsrc = x + (size_t)rb * 128 * 1280;
  int kcur = ((idx & 7) + 3 * (idx >> 3) + 5 * (xcd & 1)) % 10;
  auto stage = [&](unsigned sb) {
    const char* xs = xsrc + kcur * 128;
    const char* wsb = wsrc + (size_t)kcur * 64 * ldb;
#pragma unroll
    for (int i = 0; i < 8; ++i) glds_s(xs + i * 8 * 1280, aoff[i & 1], lbase + sb + (64 * ld + 8 * i) * 128);
#pragma unroll
    for (int i = 0; i < 16; ++i)
      glds_s(wsb + (size_t)(2 * i) * ldb, boff[(i & 1) | (((i >> 2) & 1) << 1)], lbase + sb + 16384 + (32 * ld + 2 * i) * 512);
    kcur = (kcur == 9) ? 0 : kcur + 1;
  };
  if (ld == 0) glds_s(b1, (unsigned)min(448 * max(ct - 9, 0) + lane * 16, 3056), lbase + L_BIAS);
  stage(0);
  stage(STG);
  stage(2 * STG);
  if (bid < 16) ((u32x4*)(ws + WS_KVC))[bid * 128 + ld * 64 + lane] = (u32x4){SENT, SENT, SENT, SENT};
  WAIT_VM(49);
  BARRIER();
  unsigned nb = 0;
#pragma unroll 1
  for (int k = 1; k < 8; ++k) {
    WAIT_VM(24);
    BARRIER();
    stage(nb);
    nb += STG; if (nb >= 3 * STG) nb = 0;
  }
  WAIT_VM(24);
  BARRIER();
  const int p0 = bid * 2 + ld;
  const int np = (p0 + 512 < 680) ? 2 : 1;
  for (int j = 0; j < np; ++j) {
    const int piece = p0 + 512 * j, pks = piece / 10, pcg = piece - pks * 10;
    const char* src = ((pks < 48) ? (w2 + (size_t)pks * 32 * 1280) : (wo + (size_t)(pks - 48) * 32 * 1280)) + pcg * 128;
#pragma unroll
    for (int i = 0; i < 4; ++i)
      glds_s(src + i * 8 * 1280, (unsigned)((lane >> 3) * 1280 + (lane & 7) * 16), lbase + STG + (ld * 2 + j) * 4096 + i * 1024);
  }
  if (np == 2) { WAIT_VM(8); } else { WAIT_VM(4); }
  BARRIER();
  BARRIER();
  WAIT_VM(0);
  {
    const int g = lane >> 4, q4 = (lane & 15) >> 2, p4 = lane & 3;
    for (int j = 0; j < np; ++j) {
      const int piece = p0 + 512 * j, pks = piece / 10, pcg = piece - pks * 10;
      char* dst = ws + WS_WT + (size_t)pks * 1024 + lane * 16;
      unsigned base = (unsigned)(STG + (ld * 2 + j) * 4096 + (4 * g + q4) * 128 + p4 * 8);
#pragma unroll
      for (int n = 0; n < 4; ++n) {
        s16x8 f = tr_frag(smem, base + n * 32, base + n * 32 + 2048);
        *(s16x8*)(dst + (size_t)(pcg * 4 + n) * KS_TOT * 1024) = f;
      }
    }
  }
  if (ct < 8) BARRIER();
}

constexpr int K2_KV = 61440;
constexpr int K2_ZERO = K2_KV + 4 * 6784;
constexpr int K2_LDS = K2_ZERO + 16;
constexpr int DEPTH = 5;

__global__ __launch_bounds__(256, 1) void k2(const char* __restrict__ x, const char* __restrict__ b2,
                                            char* __restrict__ ws, char* __restrict__ out) {
  __shared__ __attribute__((aligned(16))) char smem[K2_LDS];
  const int tid = threadIdx.x, lane = tid & 63;
  const int w = __builtin_amdgcn_readfirstlane(tid >> 6);
  const int bid = blockIdx.x;
  const int xcd = bid & 7, idx = bid >> 3;
  const int rtl = (xcd >> 1) * 8 + (idx & 7);
  const int cl = (xcd & 1) * 4 + (idx >> 3);
  const int g = lane >> 4, l15 = lane & 15;
  constexpr int KID = 1;
  STAMP(0);
  const char* act = ws + WS_ACT + (size_t)rtl * 4 * KS_TOT * 1024 + lane * 16;
  const char* wt = ws + WS_WT + (size_t)cl * 5 * KS_TOT * 1024 + lane * 16;
  const int ks0 = 12 * w, kso = 48 + 5 * w;
#define SB() __builtin_amdgcn_sched_barrier(0)

  s16x8 fr[17][9];
  auto ld1 = [&](auto sc, auto jc) {
    constexpr int s = decltype(sc)::value, j = decltype(jc)::value;
    if constexpr (s < 17 && (j < 5 || s < 12)) {
      const int ks = (s < 12) ? ks0 + s : kso + (s - 12);
      if constexpr (j < 5) fr[s][j] = *(const s16x8*)(wt + ((size_t)j * KS_TOT + ks) * 1024);
      else fr[s][j] = *(const s16x8*)(act + ((size_t)(j - 5) * KS_TOT + ks) * 1024);
    }
  };
  auto ldstep = [&](auto sc) {
    ld1(sc, IC<5>{}); ld1(sc, IC<6>{}); ld1(sc, IC<7>{}); ld1(sc, IC<8>{});
    ld1(sc, IC<0>{}); ld1(sc, IC<1>{}); ld1(sc, IC<2>{}); ld1(sc, IC<3>{}); ld1(sc, IC<4>{});
  };

  const int rhead = bid >> 3, rdw = (bid & 7) * 27 + min(lane, 26);
  float2 part[16];
  if (w == 0) {
    const float* src = (const float*)(ws + WS_KVP) + (size_t)rhead * 420 + min(rdw, 209) * 2;
#pragma unroll
    for (int p = 0; p < 16; ++p) part[p] = *(const float2*)(src + (size_t)p * 32 * 420);
  }
  SB();
  ldstep(IC<0>{}); ldstep(IC<1>{});
  SB();
  if (w == 0) {
    float a = 0.f, b = 0.f;
#pragma unroll
    for (int p = 0; p < 16; ++p) { a += part[p].x; b += part[p].y; }
    unsigned v = pk_bf16(a, b);
    if (v == SENT) v = 0x7FC07FC0u;
    if (lane < 27 && rdw < 210)
      __hip_atomic_store((GAS unsigned*)(ws + WS_KVC) + rhead * 256 + rdw, v, __ATOMIC_RELAXED, __HIP_MEMORY_SCOPE_AGENT);
  }
  SB();
  ldstep(IC<2>{}); ldstep(IC<3>{}); ldstep(IC<4>{});
  static_assert(DEPTH == 5, "prologue");
  if (lane < 4) *(unsigned*)(smem + K2_ZERO + lane * 4) = 0u;
  SB();

  f32x4 acc[5][4];
#pragma unroll
  for (int n = 0; n < 5; ++n)
#pragma unroll
    for (int r = 0; r < 4; ++r) acc[n][r] = (f32x4){0.f, 0.f, 0.f, 0.f};
  const char* kvb = smem + K2_KV + w * 6784;
  const f32x4 z4 = (f32x4){0.f, 0.f, 0.f, 0.f};
  const __amdgpu_buffer_rsrc_t rs = __builtin_amdgcn_make_buffer_rsrc((void*)(ws + WS_KVC), 0, 32768, 0x00020000);
  s16x8 pq[4][5];
  u32x4 kvv[8];
  f32x4 den[2][4];
  const int row = rtl * 64 + w * 16 + l15;
  u32x2 xv[5], bv[5];
  auto kvfrag = [&](int he, int ebase, int b) -> s16x8 {
    u32x2 h2[2];
#pragma unroll
    for (int hh = 0; hh < 2; ++hh) {
      int d = 32 * b + 16 * hh + 4 * g;
      int hd = d / 20;
      const char* p = (hd == he) ? (kvb + ebase + (d - 20 * hd) * 2) : (smem + K2_ZERO);
      h2[hh] = *(const u32x2*)p;
    }
    return __builtin_bit_cast(s16x8, (u32x4){h2[0][0], h2[0][1], h2[1][0], h2[1][1]});
  };
  STAMP(1);
  auto step = [&](auto sc) {
    constexpr int s = decltype(sc)::value;
    using NX = IC<s + DEPTH>;
    auto mm = [&](auto nc) {
      constexpr int n = decltype(nc)::value;
#pragma unroll
      for (int r = 0; r < 4; ++r) acc[n][r] = MFMA16(fr[s][n], fr[s][5 + r], acc[n][r]);
    };
    ld1(NX{}, IC<5>{}); ld1(NX{}, IC<6>{}); SB(); mm(IC<0>{}); SB();
    ld1(NX{}, IC<7>{}); ld1(NX{}, IC<8>{}); SB(); mm(IC<1>{}); SB();
    ld1(NX{}, IC<0>{}); ld1(NX{}, IC<1>{}); SB(); mm(IC<2>{}); SB();
    ld1(NX{}, IC<2>{}); ld1(NX{}, IC<3>{}); SB(); mm(IC<3>{}); SB();
    ld1(NX{}, IC<4>{}); SB(); mm(IC<4>{}); SB();
    if constexpr (s < 4) {
#pragma unroll
      for (int bb = 0; bb < 5; ++bb) pq[s][bb] = *(const s16x8*)(act + ((size_t)s * KS_TOT + kso + bb) * 1024);
    }
    if constexpr (s == 4) {
#pragma unroll
      for (int h = 0; h < 8; ++h) kvv[h] = __builtin_amdgcn_raw_buffer_load_b128(rs, (unsigned)((8 * w + h) * 1024 + lane * 16), 0, 16);
    }
    if constexpr (s == 6) {
      for (int spin = 0; spin < (1 << 20); ++spin) {
        bool bad = false;
#pragma unroll
        for (int h = 0; h < 8; ++h) {
          bool b01 = (kvv[h][0] == SENT) || (kvv[h][1] == SENT);
          bool b23 = (kvv[h][2] == SENT) || (kvv[h][3] == SENT);
          bad |= (lane < 52) ? (b01 || b23) : ((lane == 52) ? b01 : false);
        }
        if (!__any(bad)) break;
        __builtin_amdgcn_s_sleep(2);
#pragma unroll
        for (int h = 0; h < 8; ++h) kvv[h] = __builtin_amdgcn_raw_buffer_load_b128(rs, (unsigned)((8 * w + h) * 1024 + lane * 16), 0, 16);
      }
      if (lane < 53) {
#pragma unroll
        for (int h = 0; h < 8; ++h) *(u32x4*)(smem + K2_KV + w * 6784 + h * 848 + lane * 16) = kvv[h];
      }
#pragma unroll
      for (int c = 0; c < 2; ++c) {
        int he = 4 * c + (l15 & 3);
        int eb = he * 848 + 800;
#pragma unroll
        for (int r = 0; r < 4; ++r) den[c][r] = z4;
#pragma unroll
        for (int bb = 0; bb < 3; ++bb) {
          s16x8 zf = kvfrag(he, eb, 2 * c + bb);
#pragma unroll
          for (int r = 0; r < 4; ++r) den[c][r] = MFMA16(zf, pq[r][2 * c + bb], den[c][r]);
        }
#pragma unroll
        for (int r = 0; r < 4; ++r)
#pragma unroll
          for (int i = 0; i < 4; ++i) den[c][r][i] = __builtin_amdgcn_rcpf(den[c][r][i] + 1e-6f);
      }
    }
    if constexpr (s >= 7 && s < 12) {
      constexpr int bp = s - 7;
      unsigned ow[4][4];
#pragma unroll
      for (int hf = 0; hf < 2; ++hf) {
        const int jt = 2 * bp + hf;
        int e = 16 * jt + l15, he = e / 20, eb = he * 848 + (e - 20 * he) * 40;
        const int dlo = 20 * ((16 * jt) / 20), dhi = 20 * ((16 * jt + 15) / 20) + 19;
        const int blo = dlo / 32, bhi = dhi / 32;
        f32x4 num[4];
#pragma unroll
        for (int r = 0; r < 4; ++r) num[r] = z4;
#pragma unroll
        for (int b = 0; b < 5; ++b) {
          if (b < blo || b > bhi) continue;
          s16x8 kf = kvfrag(he, eb, b);
#pragma unroll
          for (int r = 0; r < 4; ++r) num[r] = MFMA16(kf, pq[r][b], num[r]);
        }
        const int h0 = (4 * jt) / 5, h1 = (4 * jt + 3) / 5;
        const bool hi = g >= 5 * h1 - 4 * jt;
#pragma unroll
        for (int r = 0; r < 4; ++r) {
          float rd = den[h0 >> 2][r][h0 & 3];
          if (h1 != h0) rd = hi ? den[h1 >> 2][r][h1 & 3] : rd;
          ow[r][2 * hf] = pk_bf16(num[r][0] * rd, num[r][1] * rd);
          ow[r][2 * hf + 1] = pk_bf16(num[r][2] * rd, num[r][3] * rd);
        }
      }
#pragma unroll
      for (int r = 0; r < 4; ++r) fr[12 + bp][5 + r] = __builtin_bit_cast(s16x8, (u32x4){ow[r][0], ow[r][1], ow[r][2], ow[r][3]});
    }
    if constexpr (s == 12) {
#pragma unroll
      for (int n = 0; n < 5; ++n) {
        const int col = cl * 80 + n * 16 + 4 * g;
        xv[n] = *(const u32x2*)(x + ((size_t)row * 640 + col) * 2);
        bv[n] = *(const u32x2*)(b2 + col * 2);
      }
    }
    SB();
  };
#define STEP(i) step(IC<i>{})
  STEP(0); STEP(1); STEP(2); STEP(3); STEP(4); STEP(5); STEP(6); STEP(7); STEP(8);
  STEP(9); STEP(10); STEP(11); STEP(12); STEP(13); STEP(14); STEP(15); STEP(16);
#undef STEP
  STAMP(2);

  {
    char* red = smem;
#pragma unroll
    for (int r = 0; r < 4; ++r) {
      if (r == w) continue;
      int slot = (w < r) ? w : w - 1;
#pragma unroll
      for (int n = 0; n < 5; ++n) *(f32x4*)(red + ((r * 3 + slot) * 5 + n) * 1024 + lane * 16) = acc[n][r];
    }
    asm volatile("s_waitcnt lgkmcnt(0)" ::: "memory");
    BARRIER();
    STAMP(3);
#pragma unroll
    for (int n = 0; n < 5; ++n) {
      f32x4 v = (w == 0) ? acc[n][0] : (w == 1) ? acc[n][1] : (w == 2) ? acc[n][2] : acc[n][3];
#pragma unroll
      for (int sidx = 0; sidx < 3; ++sidx) v += *(const f32x4*)(red + ((w * 3 + sidx) * 5 + n) * 1024 + lane * 16);
      const int col = cl * 80 + n * 16 + 4 * g;
      float o0 = v[0] + bf_lo(xv[n][0]) + bf_lo(bv[n][0]);
      float o1 = v[1] + bf_hi(xv[n][0]) + bf_hi(bv[n][0]);
      float o2 = v[2] + bf_lo(xv[n][1]) + bf_lo(bv[n][1]);
      float o3 = v[3] + bf_hi(xv[n][1]) + bf_hi(bv[n][1]);
      *(u32x2*)(out + ((size_t)row * 640 + col) * 2) = (u32x2){pk_bf16(o0, o1), pk_bf16(o2, o3)};
    }
  }
  STAMP(4);
}

}

extern "C" void kernel_launch(void* const* d_in, const int* in_sizes, int n_in,
                              void* d_out, int out_size, void* d_ws, size_t ws_size,
                              hipStream_t stream) {
  const char* x = (const char*)d_in[0];
  const char* wqkv = (const char*)d_in[1];
  const char* wo = (const char*)d_in[2];
  const char* w1 = (const char*)d_in[4];
  const char* b1 = (const char*)d_in[5];
  const char* w2 = (const char*)d_in[6];
  const char* b2 = (const char*)d_in[7];
  k1<<<256, 640, 0, stream>>>(x, wqkv, wo, w1, b1, w2, (char*)d_ws);
  k2<<<256, 256, 0, stream>>>(x, b2, (char*)d_ws, (char*)d_out);
}
```

```cpp
#include <hip/hip_runtime.h>
#include <hip/hip_bf16.h>
#include <stdint.h>

typedef short s16x8 __attribute__((ext_vector_type(8)));
typedef short s16x4 __attribute__((ext_vector_type(4)));
typedef float f32x4 __attribute__((ext_vector_type(4)));
typedef unsigned u32x4 __attribute__((ext_vector_type(4)));
typedef unsigned u32x2 __attribute__((ext_vector_type(2)));
typedef __bf16 bf16x2v __attribute__((ext_vector_type(2)));
#define LAS __attribute__((address_space(3)))
#define GAS __attribute__((address_space(1)))
#define DI __device__ __forceinline__

namespace {

template <int V> struct IC { static constexpr int value = V; };

constexpr int KS_TOT = 68;
constexpr size_t ACT_BYTES = (size_t)128 * KS_TOT * 1024;
constexpr size_t WT_BYTES = (size_t)40 * KS_TOT * 1024;
constexpr size_t KVP_BYTES = (size_t)16 * 32 * 420 * 4;
constexpr size_t WS_ACT = 0;
constexpr size_t WS_WT = WS_ACT + ACT_BYTES;
constexpr size_t WS_KVP = WS_WT + WT_BYTES;
constexpr size_t WS_KVC = WS_KVP + KVP_BYTES;
constexpr unsigned SENT = 0xFFFFFFFFu;

constexpr int STG = 49152;
constexpr int L_STAT = 3 * STG;
constexpr int L_CS = L_STAT + 1536;
constexpr int L_BIAS = L_CS + 896;
constexpr int L_RAW = L_BIAS + 1024;
constexpr int K1_LDS = L_RAW + 1024;

DI unsigned pk_bf16(float a, float b) {
  bf16x2v v; v[0] = (__bf16)a; v[1] = (__bf16)b;
  return __builtin_bit_cast(unsigned, v);
}
DI float bf_lo(unsigned u) { return __uint_as_float(u << 16); }
DI float bf_hi(unsigned u) { return __uint_as_float(u & 0xffff0000u); }
DI float dot2(unsigned a, unsigned b, float c) {
  return __builtin_amdgcn_fdot2_f32_bf16(__builtin_bit_cast(bf16x2v, a), __builtin_bit_cast(bf16x2v, b), c, false);
}
DI float phi_f(float t) {
  float e = __builtin_amdgcn_exp2f(t * 1.4426950408889634f);
  return (t > 0.f ? t + 1.f : e) + 1e-6f;
}
DI float gelu_f(float v) {
  const float c1 = -2.f * 0.7978845608028654f * 1.4426950408889634f;
  const float c2 = c1 * 0.044715f;
  float p = __builtin_fmaf(v * v, c2, c1);
  float e = __builtin_amdgcn_exp2f(v * p);
  return v * __builtin_amdgcn_rcpf(1.f + e);
}
DI s16x8 tr_frag(const char* smem, unsigned off0, unsigned off1) {
  s16x4 lo = __builtin_amdgcn_ds_read_tr16_b64_v4i16((LAS s16x4*)(smem + off0));
  s16x4 hi = __builtin_amdgcn_ds_read_tr16_b64_v4i16((LAS s16x4*)(smem + off1));
  return __builtin_shufflevector(lo, hi, 0, 1, 2, 3, 4, 5, 6, 7);
}
#define MFMA16(a, b, c) __builtin_amdgcn_mfma_f32_16x16x32_bf16((a), (b), (c), 0, 0, 0)
DI void glds_s(const char* sbase, unsigned voff, unsigned ldst) {
  unsigned keep;
  asm volatile("s_mov_b32 %0, m0\n\ts_mov_b32 m0, %3\n\ts_nop 0\n\tglobal_load_lds_dwordx4 %1, %2\n\ts_mov_b32 m0, %0"
               : "=&s"(keep) : "v"(voff), "s"(sbase), "s"(ldst) : "memory");
}
#define WAIT_VM(n) asm volatile("s_waitcnt vmcnt(" #n ")" ::: "memory")
#define BARRIER() do { asm volatile("" ::: "memory"); __builtin_amdgcn_s_barrier(); asm volatile("" ::: "memory"); } while (0)
#define STAMP(i)

template <int RG, bool KV>
DI void k1_loop(const char* smem, unsigned (&ard)[2], unsigned (&brd)[7], int hN,
                f32x4 (&acc)[2][7], f32x4 (&st)[2], f32x4 (&cs)[2]) {
  const s16x8 ones = __builtin_bit_cast(s16x8, (u32x4){0x3F803F80u, 0x3F803F80u, 0x3F803F80u, 0x3F803F80u});
  s16x8 a0[2], a1[2], bx[4], by[3];
  auto rdA = [&](s16x8 (&a)[2], int s) {
    a[0] = *(const LAS s16x8*)(smem + ard[s]);
    a[1] = *(const LAS s16x8*)(smem + ard[s] + 2048);
  };
  auto rdX = [&](int s) {
#pragma unroll
    for (int u = 0; u < 4; ++u) bx[u] = tr_frag(smem, brd[u] + s * 16384, brd[u] + s * 16384 + 2048);
  };
  auto rdY = [&](int s) {
#pragma unroll
    for (int u = 4; u < 7; ++u) by[u - 4] = tr_frag(smem, brd[u] + s * 16384, brd[u] + s * 16384 + 2048);
  };
  auto mm1 = [&](const s16x8 (&a)[2], const s16x8& b, int u) {
    if (KV && u < 5) {
      acc[0][u] = MFMA16(a[0], b, acc[0][u]);
      acc[1][u] = MFMA16(a[1], b, acc[1][u]);
    } else {
      acc[0][u] = MFMA16(b, a[0], acc[0][u]);
      acc[1][u] = MFMA16(b, a[1], acc[1][u]);
    }
  };
  auto mmX = [&](const s16x8 (&a)[2]) {
#pragma unroll
    for (int u = 0; u < 4; ++u) mm1(a, bx[u], u);
    cs[0] = MFMA16(ones, bx[RG], cs[0]);
  };
  auto mmY = [&](const s16x8 (&a)[2]) {
#pragma unroll
    for (int u = 4; u < 7; ++u) mm1(a, by[u - 4], u);
    if (RG < 3) cs[1] = MFMA16(ones, by[RG < 3 ? RG : 0], cs[1]);
    st[0] = MFMA16(a[0], hN ? a[0] : ones, st[0]);
    st[1] = MFMA16(a[1], hN ? a[1] : ones, st[1]);
  };
#define SBK() __builtin_amdgcn_sched_barrier(0)
#define ILV(n) do { _Pragma("unroll") for (int _i = 0; _i < (n); ++_i) { __builtin_amdgcn_sched_group_barrier(0x008, 1, 0); __builtin_amdgcn_sched_group_barrier(0x100, 1, 0); } } while (0)
  unsigned sb = 0;
  BARRIER();
  rdA(a0, 0); rdX(0);
  SBK();
#pragma unroll 1
  for (int kt = 0; kt < 10; ++kt) {
    rdY(0); mmX(a0); ILV(6); SBK();
    rdA(a1, 1); rdX(1); mmY(a0); ILV(9); SBK();
    rdY(1); mmX(a1); ILV(6);
    sb += STG;
    unsigned dl = STG;
    if (sb >= 3 * STG) { sb = 0; dl = (unsigned)(-2 * STG); }
    ard[0] += dl; ard[1] += dl;
#pragma unroll
    for (int u = 0; u < 7; ++u) brd[u] += dl;
    SBK();
    asm volatile("s_waitcnt lgkmcnt(0)" ::: "memory");
    BARRIER();
    rdA(a0, 0); rdX(0);
    mmY(a1); ILV(9); SBK();
  }
}

template <int TYPE>
DI void k1_compute(char* smem, const char* __restrict__ b1, char* __restrict__ ws, int w, int lane, int rb, int ct, int ch0) {
  constexpr int KID = 0;
  const int rg = w & 3, hN = w >> 2;
  unsigned ard[2], brd[7];
  {
    const int g = lane >> 4, l15 = lane & 15, q4 = l15 >> 2, p4 = lane & 3;
    int row = 32 * rg + l15, f = (row >> 1) & 7;
    ard[0] = (unsigned)(row * 128 + ((g ^ f) << 4));
    ard[1] = ard[0] ^ 64u;
    int sx = q4 | ((g & 1) << 2);
#pragma unroll
    for (int u = 0; u < 7; ++u) {
      int xx = 16 * u + 4 * p4, c4;
      if (TYPE == 0) {
        int ck = 2 * ch0 + 10 * hN + 4 * u + p4;
        int cv = 2 * (12 + ch0) + 10 * hN + 4 * u + p4 - 10;
        int cq = 48 + 8 * hN + 4 * u + p4 - 20;
        c4 = (u < 2) ? ck : (u == 2) ? (p4 < 2 ? ck : cv) : (u < 5) ? cv : cq;
      } else if (TYPE == 1) c4 = ((64 * hN + xx) / 4) & 31;
      else c4 = (2 * ch0 + (112 * hN + xx) / 4) & 63;
      int ch = c4 >> 1, hf = c4 & 1;
      brd[u] = (unsigned)(16384 + (8 * g + q4) * 512 + ((ch ^ (sx << 1)) << 4) + hf * 8);
    }
  }
  const int U0 = (TYPE == 0) ? 96 + 4 * ct + 2 * hN - 5 : (TYPE == 1) ? 128 + 4 * hN : 14 * (ct - 9) + 7 * hN;

  f32x4 acc[2][7];
#pragma unroll
  for (int i = 0; i < 2; ++i)
#pragma unroll
    for (int u = 0; u < 7; ++u) acc[i][u] = (f32x4){0.f, 0.f, 0.f, 0.f};
  f32x4 st[2], cs[2];
  st[0] = st[1] = cs[0] = cs[1] = (f32x4){0.f, 0.f, 0.f, 0.f};
  STAMP(1);
  if (rg == 0) k1_loop<0, TYPE == 0>(smem, ard, brd, hN, acc, st, cs);
  else if (rg == 1) k1_loop<1, TYPE == 0>(smem, ard, brd, hN, acc, st, cs);
  else if (rg == 2) k1_loop<2, TYPE == 0>(smem, ard, brd, hN, acc, st, cs);
  else k1_loop<3, TYPE == 0>(smem, ard, brd, hN, acc, st, cs);
  STAMP(2);
  lane = __builtin_amdgcn_mbcnt_hi(~0u, __builtin_amdgcn_mbcnt_lo(~0u, 0u));
  asm volatile("" : "+v"(lane));
  const int g = lane >> 4, l15 = lane & 15;

  float* st_m = (float*)(smem + L_STAT);
  float* st_r = st_m + 128;
  float* csum = (float*)(smem + L_CS);
  float* raw = (float*)(smem + L_RAW);
  if (g == (l15 >> 2)) {
#pragma unroll
    for (int rt = 0; rt < 2; ++rt) {
      float v = (l15 & 2) ? ((l15 & 1) ? st[rt][3] : st[rt][2]) : ((l15 & 1) ? st[rt][1] : st[rt][0]);
      raw[hN * 128 + 32 * rg + 16 * rt + l15] = v;
    }
  }
  if (g == 0) {
    csum[hN * 112 + rg * 16 + l15] = cs[0][0];
    if (rg < 3) csum[hN * 112 + (rg + 4) * 16 + l15] = cs[1][0];
  }
  asm volatile("s_waitcnt lgkmcnt(0)" ::: "memory");
  BARRIER();
  if (hN == 0 && lane < 32) {
    int row = 32 * rg + lane;
    float m = raw[row] * (1.f / 640.f);
    float var = fmaxf(raw[128 + row] * (1.f / 640.f) - m * m, 0.f);
    float r = __builtin_amdgcn_rsqf(var + 1e-5f);
    if (TYPE == 2) {
      float v2 = var * r * r;
      r *= __builtin_amdgcn_rsqf(v2 + 1e-5f);
    }
    st_m[row] = m; st_r[row] = r;
  }
  asm volatile("s_waitcnt lgkmcnt(0)" ::: "memory");
  BARRIER();
  STAMP(3);

  if (TYPE == 0) {
    f32x4 mv[2], rv[2];
#pragma unroll
    for (int rt = 0; rt < 2; ++rt) {
      mv[rt] = *(const f32x4*)(st_m + 32 * rg + 16 * rt + 4 * g);
      rv[rt] = *(const f32x4*)(st_r + 32 * rg + 16 * rt + 4 * g);
    }
    s16x8 fr[5];
#pragma unroll
    for (int u = 0; u < 5; ++u) {
      float c = csum[hN * 112 + u * 16 + l15];
      unsigned pw[4];
#pragma unroll
      for (int rt = 0; rt < 2; ++rt) {
        float o[4];
#pragma unroll
        for (int i = 0; i < 4; ++i) {
          float v = (acc[rt][u][i] - mv[rt][i] * c) * rv[rt][i];
          if (u < 2) o[i] = phi_f(v);
          else if (u == 2) { float ph = phi_f(v); o[i] = (l15 < 8) ? ph : v; }
          else o[i] = v;
        }
        pw[2 * rt] = pk_bf16(o[0], o[1]); pw[2 * rt + 1] = pk_bf16(o[2], o[3]);
      }
      fr[u] = __builtin_bit_cast(s16x8, (u32x4){pw[0], pw[1], pw[2], pw[3]});
    }
    const s16x8 ones = __builtin_bit_cast(s16x8, (u32x4){0x3F803F80u, 0x3F803F80u, 0x3F803F80u, 0x3F803F80u});
    const f32x4 z4 = (f32x4){0.f, 0.f, 0.f, 0.f};
    char* sc = smem + hN * 2 * STG + rg * 10240 + lane * 16;
    *(f32x4*)(sc + 0 * 1024) = MFMA16(fr[0], fr[2], z4);
    *(f32x4*)(sc + 1 * 1024) = MFMA16(fr[0], fr[3], z4);
    *(f32x4*)(sc + 2 * 1024) = MFMA16(fr[1], fr[2], z4);
    *(f32x4*)(sc + 3 * 1024) = MFMA16(fr[1], fr[3], z4);
    *(f32x4*)(sc + 4 * 1024) = MFMA16(fr[1], fr[4], z4);
    *(f32x4*)(sc + 5 * 1024) = MFMA16(fr[2], fr[3], z4);
    *(f32x4*)(sc + 6 * 1024) = MFMA16(fr[2], fr[4], z4);
    *(f32x4*)(sc + 7 * 1024) = MFMA16(fr[0], ones, z4);
    *(f32x4*)(sc + 8 * 1024) = MFMA16(fr[1], ones, z4);
    *(f32x4*)(sc + 9 * 1024) = MFMA16(fr[2], ones, z4);
  }

  {
    char* act = ws + WS_ACT;
    float mS[2], sc[2];
#pragma unroll
    for (int rt = 0; rt < 2; ++rt) {
      int row = 32 * rg + 16 * rt + l15;
      sc[rt] = st_r[row];
      mS[rt] = -st_m[row] * sc[rt];
    }
    char* abase = act + (size_t)(rb * 8 + rg * 2) * KS_TOT * 1024 + lane * 16;
#pragma unroll
    for (int u = (TYPE == 0 ? 5 : 0); u < (TYPE == 1 ? 4 : 7); ++u) {
      const int U = U0 + u;
      f32x4 c = *(const f32x4*)(csum + hN * 112 + u * 16 + 4 * g);
      f32x4 bs = (f32x4){0.f, 0.f, 0.f, 0.f};
      if (TYPE == 2) {
        u32x2 bb = *(const u32x2*)(smem + L_BIAS + (hN * 112 + u * 16 + 4 * g) * 2);
        bs = (f32x4){bf_lo(bb[0]), bf_hi(bb[0]), bf_lo(bb[1]), bf_hi(bb[1])};
      }
#pragma unroll
      for (int rt = 0; rt < 2; ++rt) {
        float o[4];
#pragma unroll
        for (int i = 0; i < 4; ++i) {
          float v = __builtin_fmaf(acc[rt][u][i], sc[rt], __builtin_fmaf(mS[rt], c[i], bs[i]));
          o[i] = (TYPE == 2) ? gelu_f(v) : phi_f(v);
        }
        u32x2 pk = (u32x2){pk_bf16(o[0], o[1]), pk_bf16(o[2], o[3])};
        if (TYPE != 2 || U < 96)
          *(u32x2*)(abase + (size_t)(rt * KS_TOT + (U >> 1)) * 1024 + (U & 1) * 8) = pk;
      }
    }
  }
  STAMP(4);
  if (TYPE == 0) {
    asm volatile("s_waitcnt lgkmcnt(0)" ::: "memory");
    BARRIER();
    const int t = w * 64 + lane;
    if (t < 420) {
      int hh = t / 105, rem = t - hh * 105, e = rem / 5, d4 = rem - e * 5;
      int hl = hh & 1;
      int dk = 20 * hl + 4 * d4, ua = dk >> 4, gg = (dk & 15) >> 2;
      int tile, ln;
      if (e == 20) { tile = 7 + ua; ln = 0; }
      else {
        int ev = 40 + 20 * hl + e, ub = ev >> 4; ln = ev & 15;
        tile = (ua == 0) ? ub - 2 : (ua == 1) ? ub : ub + 2;
      }
      const char* src = smem + (hh >> 1) * 2 * STG + tile * 1024 + (gg * 16 + ln) * 16;
      f32x4 v = *(const f32x4*)src;
#pragma unroll
      for (int r = 1; r < 4; ++r) v += *(const f32x4*)(src + r * 10240);
      float* kvp = (float*)(ws + WS_KVP) + ((size_t)rb * 32 + 4 * ct) * 420;
      ((f32x4*)kvp)[t] = v;
    }
  }
  STAMP(5);
}

__global__ __launch_bounds__(640, 3) void k1(const char* __restrict__ x, const char* __restrict__ wqkv,
                                            const char* __restrict__ wo, const char* __restrict__ w1,
                                            const char* __restrict__ b1, const char* __restrict__ w2,
                                            char* __restrict__ ws) {
  __shared__ __attribute__((aligned(16))) char smem[K1_LDS];
  const int tid = threadIdx.x, lane = tid & 63;
  const int w = __builtin_amdgcn_readfirstlane(tid >> 6);
  const int bid = blockIdx.x;
  constexpr int KID = 0;
  STAMP(0);
  const int xcd = bid & 7, idx = bid >> 3;
  const int rb = (xcd & 1) * 8 + (idx & 7);
  const int ct = (xcd >> 1) * 4 + (idx >> 3);
  int kws = 0, hL = 0, ch0 = 0;
  if (ct < 8) { int kb = 1280 + 160 * ct, L = kb & ~127; kws = L + min(kb - L, 64); ch0 = (kb - kws) >> 4; }
  else if (ct > 8) { int hb = 448 * (ct - 9); hL = hb & ~127; ch0 = (hb - hL) >> 4; }

  if (w < 8) {
    if (ct < 8) k1_compute<0>(smem, b1, ws, w, lane, rb, ct, ch0);
    else if (ct == 8) k1_compute<1>(smem, b1, ws, w, lane, rb, ct, ch0);
    else k1_compute<2>(smem, b1, ws, w, lane, rb, ct, ch0);
    return;
  }
  const int ld = w - 8;
  const unsigned lbase = (unsigned)(uintptr_t)(LAS char*)smem;
  const char* wsrc = (ct < 9) ? wqkv : w1;
  const unsigned ldb = (ct < 9) ? 3840u : 3072u;
  unsigned aoff[2], boff[4];
#pragma unroll
  for (int i = 0; i < 2; ++i) {
    int f = (4 * i + (lane >> 4)) & 7;
    aoff[i] = (unsigned)((64 * ld + (lane >> 3)) * 1280 + (((lane & 7) ^ f) << 4));
  }
#pragma unroll
  for (int v = 0; v < 4; ++v) {
    int s = ((2 * (v & 1) + (lane >> 5)) & 3) | ((v >> 1) << 2);
    int ch = (lane & 31) ^ (s << 1);
    int cb;
    if (ct < 8) cb = ch < 12 ? kws + 16 * ch : (ch < 24 ? kws + 1280 + 16 * (ch - 12) : 128 * ct + 16 * (ch - 24));
    else if (ct == 8) cb = 1024 + 16 * (ch & 15);
    else { cb = hL + 16 * ch; if (cb >= 3072) cb -= 128; }
    boff[v] = (unsigned)((32 * ld + (lane >> 5)) * ldb + cb);
  }
  const char* xsrc = x + (size_t)rb * 128 * 1280;
  int kcur = ((idx & 7) + 3 * (idx >> 3) + 5 * (xcd & 1)) % 10;
  auto stage = [&](unsigned sb) {
    const char* xs = xsrc + kcur * 128;
    const char* wsb = wsrc + (size_t)kcur * 64 * ldb;
#pragma unroll
    for (int i = 0; i < 8; ++i) glds_s(xs + i * 8 * 1280, aoff[i & 1], lbase + sb + (64 * ld + 8 * i) * 128);
#pragma unroll
    for (int i = 0; i < 16; ++i)
      glds_s(wsb + (size_t)(2 * i) * ldb, boff[(i & 1) | (((i >> 2) & 1) << 1)], lbase + sb + 16384 + (32 * ld + 2 * i) * 512);
    kcur = (kcur == 9) ? 0 : kcur + 1;
  };
  if (ld == 0) glds_s(b1, (unsigned)min(448 * max(ct - 9, 0) + lane * 16, 3056), lbase + L_BIAS);
  stage(0);
  stage(STG);
  stage(2 * STG);
  if (bid < 16) ((u32x4*)(ws + WS_KVC))[bid * 128 + ld * 64 + lane] = (u32x4){SENT, SENT, SENT, SENT};
  WAIT_VM(49);
  BARRIER();
  unsigned nb = 0;
#pragma unroll 1
  for (int k = 1; k < 8; ++k) {
    WAIT_VM(24);
    BARRIER();
    stage(nb);
    nb += STG; if (nb >= 3 * STG) nb = 0;
  }
  WAIT_VM(24);
  BARRIER();
  const int p0 = bid * 2 + ld;
  const int np = (p0 + 512 < 680) ? 2 : 1;
  for (int j = 0; j < np; ++j) {
    const int piece = p0 + 512 * j, pks = piece / 10, pcg = piece - pks * 10;
    const char* src = ((pks < 48) ? (w2 + (size_t)pks * 32 * 1280) : (wo + (size_t)(pks - 48) * 32 * 1280)) + pcg * 128;
#pragma unroll
    for (int i = 0; i < 4; ++i)
      glds_s(src + i * 8 * 1280, (unsigned)((lane >> 3) * 1280 + (lane & 7) * 16), lbase + STG + (ld * 2 + j) * 4096 + i * 1024);
  }
  if (np == 2) { WAIT_VM(8); } else { WAIT_VM(4); }
  BARRIER();
  BARRIER();
  BARRIER();
  BARRIER();
  WAIT_VM(0);
  {
    const int g = lane >> 4, q4 = (lane & 15) >> 2, p4 = lane & 3;
    for (int j = 0; j < np; ++j) {
      const int piece = p0 + 512 * j, pks = piece / 10, pcg = piece - pks * 10;
      char* dst = ws + WS_WT + (size_t)pks * 1024 + lane * 16;
      unsigned base = (unsigned)(STG + (ld * 2 + j) * 4096 + (4 * g + q4) * 128 + p4 * 8);
#pragma unroll
      for (int n = 0; n < 4; ++n) {
        s16x8 f = tr_frag(smem, base + n * 32, base + n * 32 + 2048);
        *(s16x8*)(dst + (size_t)(pcg * 4 + n) * KS_TOT * 1024) = f;
      }
    }
  }
  if (ct < 8) BARRIER();
}

constexpr int K2_KV = 61440;
constexpr int K2_ZERO = K2_KV + 4 * 6784;
constexpr int K2_LDS = K2_ZERO + 16;
constexpr int DEPTH = 5;

__global__ __launch_bounds__(256, 1) void k2(const char* __restrict__ x, const char* __restrict__ b2,
                                            char* __restrict__ ws, char* __restrict__ out) {
  __shared__ __attribute__((aligned(16))) char smem[K2_LDS];
  const int tid = threadIdx.x, lane = tid & 63;
  const int w = __builtin_amdgcn_readfirstlane(tid >> 6);
  const int bid = blockIdx.x;
  const int xcd = bid & 7, idx = bid >> 3;
  const int rtl = (xcd >> 1) * 8 + (idx & 7);
  const int cl = (xcd & 1) * 4 + (idx >> 3);
  const int g = lane >> 4, l15 = lane & 15;
  constexpr int KID = 1;
  STAMP(0);
  const char* act = ws + WS_ACT + (size_t)rtl * 4 * KS_TOT * 1024 + lane * 16;
  const char* wt = ws + WS_WT + (size_t)cl * 5 * KS_TOT * 1024 + lane * 16;
  const int ks0 = 12 * w, kso = 48 + 5 * w;
#define SB() __builtin_amdgcn_sched_barrier(0)

  s16x8 fr[17][9];
  auto ld1 = [&](auto sc, auto jc) {
    constexpr int s = decltype(sc)::value, j = decltype(jc)::value;
    if constexpr (s < 17 && (j < 5 || s < 12)) {
      const int ks = (s < 12) ? ks0 + s : kso + (s - 12);
      if constexpr (j < 5) fr[s][j] = *(const s16x8*)(wt + ((size_t)j * KS_TOT + ks) * 1024);
      else fr[s][j] = *(const s16x8*)(act + ((size_t)(j - 5) * KS_TOT + ks) * 1024);
    }
  };
  auto ldstep = [&](auto sc) {
    ld1(sc, IC<5>{}); ld1(sc, IC<6>{}); ld1(sc, IC<7>{}); ld1(sc, IC<8>{});
    ld1(sc, IC<0>{}); ld1(sc, IC<1>{}); ld1(sc, IC<2>{}); ld1(sc, IC<3>{}); ld1(sc, IC<4>{});
  };

  const int rhead = bid >> 3, rdw = (bid & 7) * 27 + min(lane, 26);
  float2 part[16];
  if (w == 0) {
    const float* src = (const float*)(ws + WS_KVP) + (size_t)rhead * 420 + min(rdw, 209) * 2;
#pragma unroll
    for (int p = 0; p < 16; ++p) part[p] = *(const float2*)(src + (size_t)p * 32 * 420);
  }
  SB();
  ldstep(IC<0>{}); ldstep(IC<1>{});
  SB();
  if (w == 0) {
    float a = 0.f, b = 0.f;
#pragma unroll
    for (int p = 0; p < 16; ++p) { a += part[p].x; b += part[p].y; }
    unsigned v = pk_bf16(a, b);
    if (v == SENT) v = 0x7FC07FC0u;
    if (lane < 27 && rdw < 210)
      __hip_atomic_store((GAS unsigned*)(ws + WS_KVC) + rhead * 256 + rdw, v, __ATOMIC_RELAXED, __HIP_MEMORY_SCOPE_AGENT);
  }
  SB();
  ldstep(IC<2>{}); ldstep(IC<3>{}); ldstep(IC<4>{});
  static_assert(DEPTH == 5, "prologue");
  if (lane < 4) *(unsigned*)(smem + K2_ZERO + lane * 4) = 0u;
  SB();

  f32x4 acc[5][4];
#pragma unroll
  for (int n = 0; n < 5; ++n)
#pragma unroll
    for (int r = 0; r < 4; ++r) acc[n][r] = (f32x4){0.f, 0.f, 0.f, 0.f};
  const char* kvb = smem + K2_KV + w * 6784;
  const f32x4 z4 = (f32x4){0.f, 0.f, 0.f, 0.f};
  const __amdgpu_buffer_rsrc_t rs = __builtin_amdgcn_make_buffer_rsrc((void*)(ws + WS_KVC), 0, 32768, 0x00020000);
  s16x8 pq[4][5];
  u32x4 kvv[8];
  f32x4 den[2][4];
  const int row = rtl * 64 + w * 16 + l15;
  u32x2 xv[5], bv[5];
  auto kvfrag = [&](int he, int ebase, int b) -> s16x8 {
    u32x2 h2[2];
#pragma unroll
    for (int hh = 0; hh < 2; ++hh) {
      int d = 32 * b + 16 * hh + 4 * g;
      int hd = d / 20;
      const char* p = (hd == he) ? (kvb + ebase + (d - 20 * hd) * 2) : (smem + K2_ZERO);
      h2[hh] = *(const u32x2*)p;
    }
    return __builtin_bit_cast(s16x8, (u32x4){h2[0][0], h2[0][1], h2[1][0], h2[1][1]});
  };
  STAMP(1);
  auto step = [&](auto sc) {
    constexpr int s = decltype(sc)::value;
    using NX = IC<s + DEPTH>;
    auto mm = [&](auto nc) {
      constexpr int n = decltype(nc)::value;
#pragma unroll
      for (int r = 0; r < 4; ++r) acc[n][r] = MFMA16(fr[s][n], fr[s][5 + r], acc[n][r]);
    };
    ld1(NX{}, IC<5>{}); ld1(NX{}, IC<6>{}); SB(); mm(IC<0>{}); SB();
    ld1(NX{}, IC<7>{}); ld1(NX{}, IC<8>{}); SB(); mm(IC<1>{}); SB();
    ld1(NX{}, IC<0>{}); ld1(NX{}, IC<1>{}); SB(); mm(IC<2>{}); SB();
    ld1(NX{}, IC<2>{}); ld1(NX{}, IC<3>{}); SB(); mm(IC<3>{}); SB();
    ld1(NX{}, IC<4>{}); SB(); mm(IC<4>{}); SB();
    if constexpr (s < 4) {
#pragma unroll
      for (int bb = 0; bb < 5; ++bb) pq[s][bb] = *(const s16x8*)(act + ((size_t)s * KS_TOT + kso + bb) * 1024);
    }
    if constexpr (s == 4) {
#pragma unroll
      for (int h = 0; h < 8; ++h) kvv[h] = __builtin_amdgcn_raw_buffer_load_b128(rs, (unsigned)((8 * w + h) * 1024 + lane * 16), 0, 16);
    }
    if constexpr (s == 6) {
      for (int spin = 0; spin < (1 << 20); ++spin) {
        bool bad = false;
#pragma unroll
        for (int h = 0; h < 8; ++h) {
          bool b01 = (kvv[h][0] == SENT) || (kvv[h][1] == SENT);
          bool b23 = (kvv[h][2] == SENT) || (kvv[h][3] == SENT);
          bad |= (lane < 52) ? (b01 || b23) : ((lane == 52) ? b01 : false);
        }
        if (!__any(bad)) break;
        __builtin_amdgcn_s_sleep(2);
#pragma unroll
        for (int h = 0; h < 8; ++h) kvv[h] = __builtin_amdgcn_raw_buffer_load_b128(rs, (unsigned)((8 * w + h) * 1024 + lane * 16), 0, 16);
      }
      if (lane < 53) {
#pragma unroll
        for (int h = 0; h < 8; ++h) *(u32x4*)(smem + K2_KV + w * 6784 + h * 848 + lane * 16) = kvv[h];
      }
#pragma unroll
      for (int c = 0; c < 2; ++c) {
        int he = 4 * c + (l15 & 3);
        int eb = he * 848 + 800;
#pragma unroll
        for (int r = 0; r < 4; ++r) den[c][r] = z4;
#pragma unroll
        for (int bb = 0; bb < 3; ++bb) {
          s16x8 zf = kvfrag(he, eb, 2 * c + bb);
#pragma unroll
          for (int r = 0; r < 4; ++r) den[c][r] = MFMA16(zf, pq[r][2 * c + bb], den[c][r]);
        }
#pragma unroll
        for (int r = 0; r < 4; ++r)
#pragma unroll
          for (int i = 0; i < 4; ++i) den[c][r][i] = __builtin_amdgcn_rcpf(den[c][r][i] + 1e-6f);
      }
    }
    if constexpr (s >= 7 && s < 12) {
      constexpr int bp = s - 7;
      unsigned ow[4][4];
#pragma unroll
      for (int hf = 0; hf < 2; ++hf) {
        const int jt = 2 * bp + hf;
        int e = 16 * jt + l15, he = e / 20, eb = he * 848 + (e - 20 * he) * 40;
        const int dlo = 20 * ((16 * jt) / 20), dhi = 20 * ((16 * jt + 15) / 20) + 19;
        const int blo = dlo / 32, bhi = dhi / 32;
        f32x4 num[4];
#pragma unroll
        for (int r = 0; r < 4; ++r) num[r] = z4;
#pragma unroll
        for (int b = 0; b < 5; ++b) {
          if (b < blo || b > bhi) continue;
          s16x8 kf = kvfrag(he, eb, b);
#pragma unroll
          for (int r = 0; r < 4; ++r) num[r] = MFMA16(kf, pq[r][b], num[r]);
        }
        const int h0 = (4 * jt) / 5, h1 = (4 * jt + 3) / 5;
        const bool hi = g >= 5 * h1 - 4 * jt;
#pragma unroll
        for (int r = 0; r < 4; ++r) {
          float rd = den[h0 >> 2][r][h0 & 3];
          if (h1 != h0) rd = hi ? den[h1 >> 2][r][h1 & 3] : rd;
          ow[r][2 * hf] = pk_bf16(num[r][0] * rd, num[r][1] * rd);
          ow[r][2 * hf + 1] = pk_bf16(num[r][2] * rd, num[r][3] * rd);
        }
      }
#pragma unroll
      for (int r = 0; r < 4; ++r) fr[12 + bp][5 + r] = __builtin_bit_cast(s16x8, (u32x4){ow[r][0], ow[r][1], ow[r][2], ow[r][3]});
    }
    if constexpr (s == 12) {
#pragma unroll
      for (int n = 0; n < 5; ++n) {
        const int col = cl * 80 + n * 16 + 4 * g;
        xv[n] = *(const u32x2*)(x + ((size_t)row * 640 + col) * 2);
        bv[n] = *(const u32x2*)(b2 + col * 2);
      }
    }
    SB();
  };
#define STEP(i) step(IC<i>{})
  STEP(0); STEP(1); STEP(2); STEP(3); STEP(4); STEP(5); STEP(6); STEP(7); STEP(8);
  STEP(9); STEP(10); STEP(11); STEP(12); STEP(13); STEP(14); STEP(15); STEP(16);
#undef STEP
  STAMP(2);

  {
    char* red = smem;
#pragma unroll
    for (int r = 0; r < 4; ++r) {
      if (r == w) continue;
      int slot = (w < r) ? w : w - 1;
#pragma unroll
      for (int n = 0; n < 5; ++n) *(f32x4*)(red + ((r * 3 + slot) * 5 + n) * 1024 + lane * 16) = acc[n][r];
    }
    asm volatile("s_waitcnt lgkmcnt(0)" ::: "memory");
    BARRIER();
    STAMP(3);
#pragma unroll
    for (int n = 0; n < 5; ++n) {
      f32x4 v = (w == 0) ? acc[n][0] : (w == 1) ? acc[n][1] : (w == 2) ? acc[n][2] : acc[n][3];
#pragma unroll
      for (int sidx = 0; sidx < 3; ++sidx) v += *(const f32x4*)(red + ((w * 3 + sidx) * 5 + n) * 1024 + lane * 16);
      const int col = cl * 80 + n * 16 + 4 * g;
      float o0 = v[0] + bf_lo(xv[n][0]) + bf_lo(bv[n][0]);
      float o1 = v[1] + bf_hi(xv[n][0]) + bf_hi(bv[n][0]);
      float o2 = v[2] + bf_lo(xv[n][1]) + bf_lo(bv[n][1]);
      float o3 = v[3] + bf_hi(xv[n][1]) + bf_hi(bv[n][1]);
      *(u32x2*)(out + ((size_t)row * 640 + col) * 2) = (u32x2){pk_bf16(o0, o1), pk_bf16(o2, o3)};
    }
  }
  STAMP(4);
}

}

extern "C" void kernel_launch(void* const* d_in, const int* in_sizes, int n_in,
                              void* d_out, int out_size, void* d_ws, size_t ws_size,
                              hipStream_t stream) {
  const char* x = (const char*)d_in[0];
  const char* wqkv = (const char*)d_in[1];
  const char* wo = (const char*)d_in[2];
  const char* w1 = (const char*)d_in[4];
  const char* b1 = (const char*)d_in[5];
  const char* w2 = (const char*)d_in[6];
  const char* b2 = (const char*)d_in[7];
  k1<<<256, 640, 0, stream>>>(x, wqkv, wo, w1, b1, w2, (char*)d_ws);
  k2<<<256, 256, 0, stream>>>(x, b2, (char*)d_ws, (char*)d_out);
}
```

```cpp
#include <hip/hip_runtime.h>
#include <hip/hip_bf16.h>
#include <stdint.h>

typedef short s16x8 __attribute__((ext_vector_type(8)));
typedef short s16x4 __attribute__((ext_vector_type(4)));
typedef float f32x4 __attribute__((ext_vector_type(4)));
typedef unsigned u32x4 __attribute__((ext_vector_type(4)));
typedef unsigned u32x2 __attribute__((ext_vector_type(2)));
typedef unsigned long long u64;
typedef __bf16 bf16x2v __attribute__((ext_vector_type(2)));
#define LAS __attribute__((address_space(3)))
#define GAS __attribute__((address_space(1)))
#define DI __device__ __forceinline__

namespace {

template <int V> struct IC { static constexpr int value = V; };

constexpr int KS_TOT = 68;
constexpr unsigned ACT_BYTES = 128u * KS_TOT * 1024;
constexpr unsigned WT_BYTES = 40u * KS_TOT * 1024;
constexpr unsigned KVP_BYTES = 16u * 32 * 420 * 4;
constexpr unsigned WS_ACT = 0;
constexpr unsigned WS_WT = WS_ACT + ACT_BYTES;
constexpr unsigned WS_KVP = WS_WT + WT_BYTES;
constexpr unsigned WS_KVC = WS_KVP + KVP_BYTES;
constexpr unsigned WS_RST = WS_KVC + 32768;
constexpr unsigned WS_CSP = WS_RST + 16 * 1024;
constexpr unsigned WS_EPOCH = WS_CSP + 256 * 1024;
constexpr unsigned WS_FLAGS = WS_EPOCH + 256;
constexpr unsigned WS_END = WS_FLAGS + 4096;
constexpr unsigned SENT = 0xFFFFFFFFu;

constexpr int L_A = 0;
constexpr int L_B = 65536;
constexpr int L_STAT = L_B + 5 * 16384;
constexpr int L_CS = L_STAT + 1024;
constexpr int L_BIAS = L_CS + 1024;
constexpr int L_CNT = L_BIAS + 1024;
constexpr int L_RET = L_CNT + 64;
constexpr int LDS_BYTES = 163840;
constexpr int P2_REG = 20480;
constexpr int KV_ZERO = 8 * 848;

struct KvTab { unsigned short v[6][64][8]; };
constexpr KvTab make_kvtab() {
  KvTab t{};
  for (int lane = 0; lane < 64; ++lane) {
    const int g = lane >> 4, l15 = lane & 15;
    int f = 0;
    for (int c = 0; c < 2; ++c) {
      for (int k = 0; k < 8; ++k) {
        int he = 0, eb = 0, blo = 0, bhi = 0;
        if (k < 3) { he = 4 * c + (l15 & 3); eb = he * 848 + 800; blo = bhi = 2 * c + k; }
        else {
          const int jt = 5 * c + k - 3, e = 16 * jt + l15;
          he = e / 20; eb = he * 848 + (e - 20 * he) * 40;
          blo = (20 * ((16 * jt) / 20)) / 32; bhi = (20 * ((16 * jt + 15) / 20) + 19) / 32;
        }
        for (int b = blo; b <= bhi; ++b) {
          for (int hh = 0; hh < 2; ++hh) {
            const int d = 32 * b + 16 * hh + 4 * g, hd = d / 20;
            const int off = (hd == he) ? eb + (d - 20 * hd) * 2 : KV_ZERO;
            const int idx = 2 * f + hh;
            t.v[idx >> 3][lane][idx & 7] = (unsigned short)off;
          }
          ++f;
        }
      }
    }
  }
  return t;
}
__device__ const KvTab g_kvtab = make_kvtab();

DI unsigned pk_bf16(float a, float b) {
  bf16x2v v; v[0] = (__bf16)a; v[1] = (__bf16)b;
  return __builtin_bit_cast(unsigned, v);
}
DI float bf_lo(unsigned u) { return __uint_as_float(u << 16); }
DI float bf_hi(unsigned u) { return __uint_as_float(u & 0xffff0000u); }
DI float dot2(unsigned a, unsigned b, float c) {
  return __builtin_amdgcn_fdot2_f32_bf16(__builtin_bit_cast(bf16x2v, a), __builtin_bit_cast(bf16x2v, b), c, false);
}
template <int CTRL> DI float dpp_add(float v) {
  return v + __int_as_float(__builtin_amdgcn_update_dpp(0, __float_as_int(v), CTRL, 0xF, 0xF, true));
}
DI float swz16_add(float v) { return v + __int_as_float(__builtin_amdgcn_ds_swizzle(__float_as_int(v), 0x401F)); }
DI float phi_f(float t) {
  float e = __builtin_amdgcn_exp2f(t * 1.4426950408889634f);
  return (t > 0.f ? t + 1.f : e) + 1e-6f;
}
DI float gelu_f(float v) {
  const float c1 = -2.f * 0.7978845608028654f * 1.4426950408889634f;
  const float c2 = c1 * 0.044715f;
  float p = __builtin_fmaf(v * v, c2, c1);
  float e = __builtin_amdgcn_exp2f(v * p);
  return v * __builtin_amdgcn_rcpf(1.f + e);
}
DI s16x8 tr_frag(const char* smem, unsigned off0, unsigned off1) {
  s16x4 lo = __builtin_amdgcn_ds_read_tr16_b64_v4i16((LAS s16x4*)(smem + off0));
  s16x4 hi = __builtin_amdgcn_ds_read_tr16_b64_v4i16((LAS s16x4*)(smem + off1));
  return __builtin_shufflevector(lo, hi, 0, 1, 2, 3, 4, 5, 6, 7);
}
#define MFMA16(a, b, c) __builtin_amdgcn_mfma_f32_16x16x32_bf16((a), (b), (c), 0, 0, 0)
DI void glds_s(const char* sbase, unsigned voff, unsigned ldst) {
  unsigned keep;
  asm volatile("s_mov_b32 %0, m0\n\ts_mov_b32 m0, %3\n\ts_nop 0\n\tglobal_load_lds_dwordx4 %1, %2\n\ts_mov_b32 m0, %0"
               : "=&s"(keep) : "v"(voff), "s"(sbase), "s"(ldst) : "memory");
}
#define WAIT_VM(n) asm volatile("s_waitcnt vmcnt(" #n ")" ::: "memory")
#define WAIT_LGKM0() asm volatile("s_waitcnt lgkmcnt(0)" ::: "memory")
#define BARRIER() do { asm volatile("" ::: "memory"); __builtin_amdgcn_s_barrier(); asm volatile("" ::: "memory"); } while (0)
#define SB() __builtin_amdgcn_sched_barrier(0)
#define ILV(n) do { _Pragma("unroll") for (int _i = 0; _i < (n); ++_i) { __builtin_amdgcn_sched_group_barrier(0x008, 1, 0); __builtin_amdgcn_sched_group_barrier(0x100, 1, 0); } } while (0)
#define LD16S(rs, voff, soff) __builtin_amdgcn_raw_buffer_load_b128((rs), (voff), (soff), 16)
#define ST16(v, rs, off) __builtin_amdgcn_raw_buffer_store_b128((v), (rs), (off), 0, 16)
typedef __amdgpu_buffer_rsrc_t rsrc_t;

DI int c4_of(int type, int ch0, int hN, int u, int p) {
  if (type == 0) {
    const int x = 4 * u + p;
    return x < 10 ? 2 * ch0 + 10 * hN + x : (x < 20 ? 2 * (12 + ch0) + 10 * hN + x - 10 : 48 + 8 * hN + x - 20);
  }
  return type == 1 ? ((16 * hN + 4 * u + p) & 31) : ((2 * ch0 + 28 * hN + 4 * u + p) & 63);
}

template <bool KV>
DI void p1_mm(const s16x8 (&a)[4], const s16x8 (&b)[7], f32x4 (&acc)[4][7]) {
#pragma unroll
  for (int u = 0; u < 7; ++u)
#pragma unroll
    for (int r = 0; r < 4; ++r)
      acc[r][u] = (KV && u < 5) ? MFMA16(a[r], b[u], acc[r][u]) : MFMA16(b[u], a[r], acc[r][u]);
}

template <bool KV>
DI void p1_loop(char* smem, char* __restrict__ ws, int w, int lane, unsigned (&ard)[2], unsigned (&brd)[7]) {
  f32x4 acc[4][7];
#pragma unroll
  for (int i = 0; i < 4; ++i)
#pragma unroll
    for (int u = 0; u < 7; ++u) acc[i][u] = (f32x4){0.f, 0.f, 0.f, 0.f};
  s16x8 a0[4], b0[7], a1[4], b1[7];
  unsigned bs = 0;
  auto rd = [&](s16x8 (&a)[4], s16x8 (&b)[7], int s) {
#pragma unroll
    for (int r = 0; r < 4; ++r) a[r] = *(const LAS s16x8*)(smem + ard[s] + r * 2048);
#pragma unroll
    for (int u = 0; u < 7; ++u) b[u] = tr_frag(smem, brd[u], brd[u] + 2048);
    unsigned dl = 16384u;
    if (++bs == 5) { bs = 0; dl = (unsigned)(-4 * 16384); }
#pragma unroll
    for (int u = 0; u < 7; ++u) brd[u] += dl;
  };
#define PIN_ACC() asm volatile("" : "+v"(acc[0][0]), "+v"(acc[0][1]), "+v"(acc[0][2]), "+v"(acc[0][3]), "+v"(acc[0][4]), "+v"(acc[0][5]), "+v"(acc[0][6]), \
    "+v"(acc[1][0]), "+v"(acc[1][1]), "+v"(acc[1][2]), "+v"(acc[1][3]), "+v"(acc[1][4]), "+v"(acc[1][5]), "+v"(acc[1][6]), \
    "+v"(acc[2][0]), "+v"(acc[2][1]), "+v"(acc[2][2]), "+v"(acc[2][3]), "+v"(acc[2][4]), "+v"(acc[2][5]), "+v"(acc[2][6]), \
    "+v"(acc[3][0]), "+v"(acc[3][1]), "+v"(acc[3][2]), "+v"(acc[3][3]), "+v"(acc[3][4]), "+v"(acc[3][5]), "+v"(acc[3][6]))
  BARRIER();
  rd(a0, b0, 0);
#pragma unroll 1
  for (int t = 0; t < 10; ++t) {
    WAIT_LGKM0();
    if (t == 1) WAIT_VM(0);
    BARRIER();
    rd(a1, b1, 1); p1_mm<KV>(a0, b0, acc); ILV(18);
    {
      unsigned da = ((t & 3) == 3) ? (unsigned)(-3 * 16384) : 16384u;
      ard[0] += da; ard[1] += da;
    }
    PIN_ACC(); SB();
    WAIT_LGKM0();
    BARRIER();
    rd(a0, b0, 0);
    p1_mm<KV>(a1, b1, acc); ILV(18);
    PIN_ACC(); SB();
  }
#pragma unroll
  for (int r = 0; r < 4; ++r)
#pragma unroll
    for (int u = 0; u < 7; ++u) *(f32x4*)(smem + ((w * 4 + r) * 7 + u) * 1024 + lane * 16) = acc[r][u];
}

DI void post_flag(char* ws, int rb, int ct, u64 val, int lane) {
  if (lane < 2)
    __hip_atomic_store((GAS u64*)(ws + WS_FLAGS) + (lane ? 256 + ct * 16 + rb : rb * 16 + ct), val, __ATOMIC_RELAXED, __HIP_MEMORY_SCOPE_AGENT);
}
DI void wait_flag(rsrc_t rs, u64 tag, unsigned foff, unsigned need) {
#pragma unroll 1
  for (int spin = 0; spin < (1 << 19); ++spin) {
    u32x2 f = __builtin_amdgcn_raw_buffer_load_b64(rs, foff, WS_FLAGS, 16);
    u64 d = (((u64)f[1] << 32) | f[0]) - tag;
    if (__all(d >= need && d <= 3)) return;
    __builtin_amdgcn_s_sleep(2);
  }
}

#define P2_COMMON \
  const unsigned act = WS_ACT + (unsigned)rtl * 4 * KS_TOT * 1024;       \
  const unsigned wt = WS_WT + (unsigned)cl * 5 * KS_TOT * 1024; \
  const unsigned vo = lane * 16;

DI void p2_hwave(char* __restrict__ ws, rsrc_t rs, u64 tag, int w, int lane, int rtl, int cl, f32x4 (&acc)[5][4]) {
  P2_COMMON
  const int ks0 = 12 + 9 * (w - 4);
  s16x8 fr[9][9];
  auto ld1 = [&](auto sc, auto jc) {
    constexpr int s = decltype(sc)::value, j = decltype(jc)::value;
    if constexpr (s < 9) {
      if constexpr (j < 5) fr[s][j] = __builtin_bit_cast(s16x8, LD16S(rs, vo, wt + (unsigned)(j * KS_TOT + ks0 + s) * 1024));
      else fr[s][j] = __builtin_bit_cast(s16x8, LD16S(rs, vo, act + (unsigned)((j - 5) * KS_TOT + ks0 + s) * 1024));
    }
  };
  auto ldw = [&](auto sc) { ld1(sc, IC<0>{}); ld1(sc, IC<1>{}); ld1(sc, IC<2>{}); ld1(sc, IC<3>{}); ld1(sc, IC<4>{}); };
  auto lda = [&](auto sc) { ld1(sc, IC<5>{}); ld1(sc, IC<6>{}); ld1(sc, IC<7>{}); ld1(sc, IC<8>{}); };
  ldw(IC<0>{}); ldw(IC<1>{}); ldw(IC<2>{}); ldw(IC<3>{}); ldw(IC<4>{});
  SB();
  if (w == 4) wait_flag(rs, tag, (unsigned)((rtl >> 1) * 16 + (lane & 15)) * 8, 2);
  BARRIER();
  lda(IC<0>{}); lda(IC<1>{}); lda(IC<2>{});
  SB();
  auto step = [&](auto sc) {
    constexpr int s = decltype(sc)::value;
    using NX = IC<s + 3>;
    using NW = IC<(s == 0) ? 99 : s + 4>;
    auto mm = [&](auto nc) {
      constexpr int n = decltype(nc)::value;
#pragma unroll
      for (int r = 0; r < 4; ++r) acc[n][r] = MFMA16(fr[s][n], fr[s][5 + r], acc[n][r]);
    };
    ld1(NX{}, IC<5>{}); ld1(NX{}, IC<6>{}); SB(); mm(IC<0>{}); SB();
    ld1(NX{}, IC<7>{}); ld1(NX{}, IC<8>{}); SB(); mm(IC<1>{}); SB();
    ld1(NW{}, IC<0>{}); ld1(NW{}, IC<1>{}); SB(); mm(IC<2>{}); SB();
    ld1(NW{}, IC<2>{}); ld1(NW{}, IC<3>{}); SB(); mm(IC<3>{}); SB();
    ld1(NW{}, IC<4>{}); SB(); mm(IC<4>{}); SB();
  };
  step(IC<0>{}); step(IC<1>{}); step(IC<2>{}); step(IC<3>{}); step(IC<4>{});
  step(IC<5>{}); step(IC<6>{}); step(IC<7>{}); step(IC<8>{});
}

DI void p2_owave(char* smem, char* __restrict__ ws, rsrc_t rs, int w, int lane, int rtl, int cl, f32x4 (&acc)[5][4]) {
  P2_COMMON
  const int g = lane >> 4;
  const int ksh = 3 * w, kso = 48 + 5 * w;
  s16x8 fr[8][9];
  auto ld1 = [&](auto sc, auto jc) {
    constexpr int s = decltype(sc)::value, j = decltype(jc)::value;
    if constexpr (s < 8 && (j < 5 || s < 3)) {
      const int ks = s < 3 ? ksh + s : kso + s - 3;
      if constexpr (j < 5) fr[s][j] = __builtin_bit_cast(s16x8, LD16S(rs, vo, wt + (unsigned)(j * KS_TOT + ks) * 1024));
      else fr[s][j] = __builtin_bit_cast(s16x8, LD16S(rs, vo, act + (unsigned)((j - 5) * KS_TOT + ks) * 1024));
    }
  };
  auto ldw = [&](auto sc) { ld1(sc, IC<0>{}); ld1(sc, IC<1>{}); ld1(sc, IC<2>{}); ld1(sc, IC<3>{}); ld1(sc, IC<4>{}); };
  auto lda = [&](auto sc) { ld1(sc, IC<5>{}); ld1(sc, IC<6>{}); ld1(sc, IC<7>{}); ld1(sc, IC<8>{}); };
  auto mmstep = [&](auto sc) {
    constexpr int s = decltype(sc)::value;
#pragma unroll
    for (int n = 0; n < 5; ++n)
#pragma unroll
      for (int r = 0; r < 4; ++r) acc[n][r] = MFMA16(fr[s][n], fr[s][5 + r], acc[n][r]);
    SB();
  };
  ldw(IC<0>{}); ldw(IC<1>{}); ldw(IC<2>{});
  SB();
  BARRIER();
  lda(IC<0>{}); lda(IC<1>{}); lda(IC<2>{});
  SB();
  s16x8 pq[4][5];
  auto ldpq = [&](int b0, int b1) {
#pragma unroll
    for (int bb = b0; bb < b1; ++bb)
#pragma unroll
      for (int r = 0; r < 4; ++r) pq[r][bb] = __builtin_bit_cast(s16x8, LD16S(rs, vo, act + (unsigned)(r * KS_TOT + kso + bb) * 1024));
  };
  char* kvb = smem + w * P2_REG;
  {
    u32x4 kvv[8];
#pragma unroll
    for (int h = 0; h < 8; ++h) kvv[h] = LD16S(rs, vo, WS_KVC + (unsigned)((8 * w + h) * 1024));
    SB();
    mmstep(IC<0>{});
    const unsigned m01 = lane < 53 ? SENT : 0u, m23 = lane < 52 ? SENT : 0u;
#pragma unroll 1
    for (int spin = 0; spin < (1 << 19); ++spin) {
      bool bad = false;
#pragma unroll
      for (int h = 0; h < 8; ++h)
        bad |= ((kvv[h][0] & m01) == SENT) || ((kvv[h][1] & m01) == SENT) || ((kvv[h][2] & m23) == SENT) || ((kvv[h][3] & m23) == SENT);
      if (!__any(bad)) break;
      __builtin_amdgcn_s_sleep(2);
#pragma unroll
      for (int h = 0; h < 8; ++h) kvv[h] = LD16S(rs, vo, WS_KVC + (unsigned)((8 * w + h) * 1024));
    }
    if (lane < 53) {
#pragma unroll
      for (int h = 0; h < 8; ++h) *(u32x4*)(kvb + h * 848 + lane * 16) = kvv[h];
    }
    if (lane == 63) *(u32x2*)(kvb + KV_ZERO) = (u32x2){0u, 0u};
  }
  u32x4 tab[3];
  auto ldtab = [&](int c) {
#pragma unroll
    for (int i = 0; i < 3; ++i) tab[i] = *(const u32x4*)&g_kvtab.v[3 * c + i][lane][0];
  };
  ldpq(0, 3); ldtab(0); SB();
  mmstep(IC<1>{});
  ldw(IC<3>{}); SB();
  mmstep(IC<2>{});
  const f32x4 z4 = (f32x4){0.f, 0.f, 0.f, 0.f};
  auto kvfrag = [&](int f) -> s16x8 {
    const unsigned o2 = tab[f >> 2][f & 3];
    u32x2 lo = *(const u32x2*)(kvb + (o2 & 0xffffu)), hi = *(const u32x2*)(kvb + (o2 >> 16));
    return __builtin_bit_cast(s16x8, (u32x4){lo[0], lo[1], hi[0], hi[1]});
  };
  unsigned ow[4][20];
  auto ohalf = [&](auto cc) {
    constexpr int c = decltype(cc)::value;
    f32x4 den[4];
#pragma unroll
    for (int r = 0; r < 4; ++r) den[r] = z4;
#pragma unroll
    for (int bb = 0; bb < 3; ++bb) {
      s16x8 zf = kvfrag(bb);
#pragma unroll
      for (int r = 0; r < 4; ++r) den[r] = MFMA16(zf, pq[r][2 * c + bb], den[r]);
    }
#pragma unroll
    for (int r = 0; r < 4; ++r)
#pragma unroll
      for (int i = 0; i < 4; ++i) den[r][i] = __builtin_amdgcn_rcpf(den[r][i] + 1e-6f);
    int f = 3;
#pragma unroll
    for (int jt = 5 * c; jt < 5 * c + 5; ++jt) {
      const int blo = (20 * ((16 * jt) / 20)) / 32, bhi = (20 * ((16 * jt + 15) / 20) + 19) / 32;
      f32x4 num[4];
#pragma unroll
      for (int r = 0; r < 4; ++r) num[r] = z4;
#pragma unroll
      for (int b = 0; b < 5; ++b) {
        if (b < blo || b > bhi) continue;
        s16x8 kf = kvfrag(f++);
#pragma unroll
        for (int r = 0; r < 4; ++r) num[r] = MFMA16(kf, pq[r][b], num[r]);
      }
      const int h0 = (4 * jt) / 5, h1 = (4 * jt + 3) / 5;
      const bool hi = g >= 5 * h1 - 4 * jt;
#pragma unroll
      for (int r = 0; r < 4; ++r) {
        float rd = den[r][h0 & 3];
        if (h1 != h0) rd = hi ? den[r][h1 & 3] : rd;
        ow[r][2 * jt] = pk_bf16(num[r][0] * rd, num[r][1] * rd);
        ow[r][2 * jt + 1] = pk_bf16(num[r][2] * rd, num[r][3] * rd);
      }
    }
    SB();
  };
  auto setof = [&](auto bc) {
    constexpr int bp = decltype(bc)::value;
#pragma unroll
    for (int r = 0; r < 4; ++r)
      fr[3 + bp][5 + r] = __builtin_bit_cast(s16x8, (u32x4){ow[r][4 * bp], ow[r][4 * bp + 1], ow[r][4 * bp + 2], ow[r][4 * bp + 3]});
  };
  ohalf(IC<0>{});
  ldw(IC<4>{}); ldpq(3, 5); ldtab(1); SB();
  setof(IC<0>{}); mmstep(IC<3>{});
  ldw(IC<5>{}); SB();
  setof(IC<1>{}); mmstep(IC<4>{});
  ohalf(IC<1>{});
  ldw(IC<6>{}); ldw(IC<7>{}); SB();
  setof(IC<2>{}); mmstep(IC<5>{});
  setof(IC<3>{}); mmstep(IC<6>{});
  setof(IC<4>{}); mmstep(IC<7>{});
}

__global__ __launch_bounds__(512, 2) void fused(const char* __restrict__ x, const char* __restrict__ wqkv,
                                               const char* __restrict__ wo, const char* __restrict__ w1,
                                               const char* __restrict__ b1, const char* __restrict__ w2,
                                               const char* __restrict__ b2, char* __restrict__ ws,
                                               char* __restrict__ out) {
  __shared__ __attribute__((aligned(16))) char smem[LDS_BYTES];
  const int tid = threadIdx.x, lane = tid & 63;
  const int w = __builtin_amdgcn_readfirstlane(tid >> 6);
  const int bid = blockIdx.x;
  const rsrc_t rs = __builtin_amdgcn_make_buffer_rsrc((void*)ws, 0, WS_END, 0x00020000);
  const unsigned lbase = (unsigned)(uintptr_t)(LAS char*)smem;
  const int xcd = bid & 7, idx = bid >> 3;
  const int rb = (xcd & 1) * 8 + (idx & 7);
  const int ct = (xcd >> 1) + 4 * (idx >> 3);
  const int type = ct < 8 ? 0 : (ct == 8 ? 1 : 2);
  const int g = lane >> 4, l15 = lane & 15;
  u64 tag;
  auto read_tag = [&]() {
    asm volatile("s_load_dwordx2 %0, %1, 0x0\n\ts_waitcnt lgkmcnt(0)" : "=s"(tag) : "s"(ws + WS_EPOCH) : "memory");
    tag += 16;
  };
  int wb0, ch0;
  if (type == 0) { int kb = 1280 + 160 * ct, L = kb & ~127; wb0 = L + min(kb - L, 64); ch0 = (kb - wb0) >> 4; }
  else { int hb = (type == 1) ? 1024 : 448 * (ct - 9); wb0 = hb & ~127; ch0 = (hb - wb0) >> 4; }
  auto chunk_off = [&](int ch) -> int {
    int cb = wb0 + 16 * ch;
    if (type == 0) cb = ch < 12 ? cb : (ch < 24 ? cb + 1280 - 192 : 128 * ct + 16 * (ch - 24));
    else if (type == 1) cb = wb0 + 16 * (ch & 15);
    else if (cb >= 3072) cb -= 128;
    return cb;
  };

  if (w < 4) {
    const char* wsrc = (type < 2) ? wqkv : w1;
    const unsigned ldb = (type < 2) ? 3840u : 3072u;
    u32x4 xr[3], wr[5];
    {
      const char* xrow = x + (size_t)(rb * 128 + 8 * ct + 2 * w + (lane >> 5)) * 1280;
      xr[0] = *(const u32x4*)(xrow + (lane & 31) * 16);
      xr[1] = *(const u32x4*)(xrow + 512 + (lane & 31) * 16);
      xr[2] = *(const u32x4*)(xrow + 1024 + (lane & 15) * 16);
    }
    const int piece = min(bid * 3 + w, 679);
    const bool do_ret = (w < 3) && (bid * 3 + w < 680);
    const int pks = piece / 10, pcg = piece - pks * 10;
    if (w < 3) {
      const char* src = ((pks < 48) ? (w2 + (size_t)pks * 32 * 1280) : (wo + (size_t)(pks - 48) * 32 * 1280)) + pcg * 128;
#pragma unroll 1
      for (int i = 0; i < 4; ++i)
        glds_s(src + i * 8 * 1280, (unsigned)((lane >> 3) * 1280 + (lane & 7) * 16), lbase + L_RET + w * 4096 + i * 1024);
    } else {
      const char* pc;
      asm volatile("s_getpc_b64 %0" : "=s"(pc));
      if (idx < 23) glds_s((const char*)((uintptr_t)pc & ~(uintptr_t)127) + idx * 1024, (unsigned)lane * 16, lbase + L_STAT);
    }
    {
      const char* wrow = wsrc + (size_t)(40 * rb + (lane >> 3)) * ldb + chunk_off(8 * w + (lane & 7));
#pragma unroll
      for (int i = 0; i < 5; ++i) wr[i] = *(const u32x4*)(wrow + (size_t)(8 * i) * ldb);
    }
    SB();
    const int mg = w >> 1, hN = w & 1, q4 = l15 >> 2, p4 = lane & 3;
    unsigned ard[2], brd[7];
    const int row = 64 * mg + l15;
    ard[0] = (unsigned)(L_A + row * 128 + ((g ^ ((row >> 1) & 7)) << 4));
    ard[1] = ard[0] ^ 64u;
    const int sx = q4 | ((g & 1) << 2);
#pragma unroll
    for (int u = 0; u < 7; ++u) {
      const int c4 = c4_of(type, ch0, hN, u, p4);
      brd[u] = (unsigned)(L_B + (8 * g + q4) * 512 + (((c4 >> 1) ^ (sx << 1)) << 4) + (c4 & 1) * 8);
    }
    read_tag();
    {
      const int rhead = bid >> 3, rdw = (bid & 7) * 27 + lane;
      if (w == 3 && lane < 27 && rdw < 210)
        __hip_atomic_store((GAS unsigned*)(ws + WS_KVC) + rhead * 256 + rdw, SENT, __ATOMIC_RELAXED, __HIP_MEMORY_SCOPE_AGENT);
      if (type == 0) {
#pragma unroll 1
        for (int it = 0; it < 2; ++it) {
          const int t = it * 256 + w * 64 + lane;
          if (t < 420) ST16(((u32x4){SENT, SENT, SENT, SENT}), rs, WS_KVP + (unsigned)((rb * 32 + 4 * ct) * 420 + t * 4) * 4);
        }
      }
    }
    SB();
    {
      const unsigned ONES = 0x3F803F80u;
      if ((lane & 31) >= 16) xr[2] = (u32x4){0u, 0u, 0u, 0u};
      float s1 = 0.f, s2 = 0.f;
#pragma unroll
      for (int i = 0; i < 3; ++i)
#pragma unroll
        for (int j = 0; j < 4; ++j) s1 = dot2(xr[i][j], ONES, s1);
      s1 = dpp_add<0xB1>(s1); s1 = dpp_add<0x4E>(s1); s1 = dpp_add<0x141>(s1); s1 = dpp_add<0x140>(s1); s1 = swz16_add(s1);
      const float mean = s1 * (1.f / 640.f);
#pragma unroll
      for (int i = 0; i < 3; ++i)
#pragma unroll
        for (int j = 0; j < 4; ++j) {
          float d0 = bf_lo(xr[i][j]) - mean, d1 = bf_hi(xr[i][j]) - mean;
          float t2 = __builtin_fmaf(d0, d0, d1 * d1);
          s2 += (i < 2 || (lane & 31) < 16) ? t2 : 0.f;
        }
      s2 = dpp_add<0xB1>(s2); s2 = dpp_add<0x4E>(s2); s2 = dpp_add<0x141>(s2); s2 = dpp_add<0x140>(s2); s2 = swz16_add(s2);
      if ((lane & 31) < 2)
        __hip_atomic_store((GAS float*)(ws + WS_RST) + rb * 256 + (lane & 1) * 128 + 8 * ct + 2 * w + (lane >> 5),
                           (lane & 1) ? s2 * (1.f / 640.f) : mean, __ATOMIC_RELAXED, __HIP_MEMORY_SCOPE_AGENT);
      unsigned SEL_LO = 0x00003F80u, SEL_HI = 0x3F800000u;
      asm volatile("" : "+v"(SEL_LO), "+v"(SEL_HI));
      f32x4 c0, c1;
#pragma unroll
      for (int j = 0; j < 4; ++j) {
        float lo = 0.f, hi = 0.f;
#pragma unroll
        for (int i = 0; i < 5; ++i) { lo = dot2(wr[i][j], SEL_LO, lo); hi = dot2(wr[i][j], SEL_HI, hi); }
        if (j < 2) { c0[2 * j] = lo; c0[2 * j + 1] = hi; } else { c1[2 * j - 4] = lo; c1[2 * j - 3] = hi; }
      }
#pragma unroll
      for (int j = 0; j < 4; ++j) {
        c0[j] = swz16_add(dpp_add<0x128>(c0[j])); c1[j] = swz16_add(dpp_add<0x128>(c1[j]));
        c0[j] += __shfl_xor(c0[j], 32); c1[j] += __shfl_xor(c1[j], 32);
      }
      if (lane < 16)
        ST16(__builtin_bit_cast(u32x4, (lane < 8) ? c0 : c1), rs,
             WS_CSP + (unsigned)(ct * 16 + rb) * 1024 + w * 256 + (lane & 7) * 32 + (lane >> 3) * 16);
    }
    if (do_ret) {
      unsigned base = (unsigned)(L_RET + w * 4096 + (4 * g + q4) * 128 + p4 * 8);
#pragma unroll 1
      for (int n = 0; n < 4; ++n) {
        s16x8 f = tr_frag(smem, base + n * 32, base + n * 32 + 2048);
        ST16(__builtin_bit_cast(u32x4, f), rs, WS_WT + (unsigned)((pcg * 4 + n) * KS_TOT + pks) * 1024 + lane * 16);
      }
    }
    if (type == 0) p1_loop<true>(smem, ws, w, lane, ard, brd);
    else p1_loop<false>(smem, ws, w, lane, ard, brd);
  } else {
    const int ld = w - 4;
    const char* xsrc = x + (size_t)rb * 128 * 1280;
    const char* wsrc = (type < 2) ? wqkv : w1;
    const unsigned ldb = (type < 2) ? 3840u : 3072u;
    const int krot2 = ((idx ^ (idx >> 3)) & 1) * 10;
    unsigned aoff[2], boff[2];
#pragma unroll
    for (int i = 0; i < 2; ++i) {
      aoff[i] = (unsigned)((16 * ld + 8 * i + (lane >> 3)) * 1280 + (((lane & 7) ^ ((4 * i + (lane >> 4)) & 7)) << 4));
      const int s = ((2 * i + (lane >> 5)) & 3) | ((ld & 1) << 2);
      boff[i] = (unsigned)((8 * ld + 2 * i + (lane >> 5)) * ldb + chunk_off((lane & 31) ^ (s << 1)));
    }
    auto issueB = [&](int j, unsigned slot) {
      const int jr = (j + krot2 >= 20) ? j + krot2 - 20 : j + krot2;
      const char* src = wsrc + (size_t)(jr * 32) * ldb;
      const unsigned d = lbase + L_B + slot * 16384 + ld * 4096;
      glds_s(src, boff[0], d); glds_s(src, boff[1], d + 1024);
      glds_s(src + 4 * ldb, boff[0], d + 2048); glds_s(src + 4 * ldb, boff[1], d + 3072);
    };
    auto issueA = [&](int t, int h) {
      const int tr = (2 * t + krot2 >= 20) ? t + krot2 / 2 - 10 : t + krot2 / 2;
      const char* src = xsrc + tr * 128 + h * 64 * 1280;
      const unsigned d = lbase + L_A + (t & 3) * 16384 + h * 8192 + ld * 2048;
      glds_s(src, aoff[0], d); glds_s(src, aoff[1], d + 1024);
    };
    issueA(0, 0); issueA(0, 1);
#pragma unroll 1
    for (int j = 0; j < 4; ++j) { issueB(j, (unsigned)j); issueA((j + 2) >> 1, j & 1); }
    if (ld == 3) glds_s(b1, (unsigned)min(448 * max(ct - 9, 0) + lane * 16, 3056), lbase + L_BIAS);
    if (lane == 0) *(unsigned*)(smem + L_CNT) = 0u;
    read_tag();
    WAIT_VM(20);
    BARRIER();
    auto stepm = [&](int m) {
      if (m + 4 < 20) issueB(m + 4, (unsigned)((m + 4) % 5));
      if (m < 14) issueA((m + 6) >> 1, m & 1);
    };
    stepm(0);
    WAIT_VM(20); BARRIER(); stepm(1);
#pragma unroll 1
    for (int m = 2; m < 13; ++m) {
      WAIT_VM(18);
      BARRIER();
      stepm(m);
      if (m == 4 && ld == 0) post_flag(ws, rb, ct, tag + 1, lane);
    }
    const unsigned fo0 = (unsigned)((lane & 16) ? 256 + ct * 16 + l15 : rb * 16 + l15) * 8;
    const unsigned fo1 = (unsigned)(ld * 64 + lane) * 8;
    u32x2 f0, f1;
    WAIT_VM(18); BARRIER(); stepm(13);
    WAIT_VM(18); BARRIER();
    asm volatile("global_load_dwordx2 %0, %2, %4 sc1\n\tglobal_load_dwordx2 %1, %3, %4 sc1"
                 : "=&v"(f0), "=&v"(f1) : "v"(fo0), "v"(fo1), "s"(ws + WS_FLAGS) : "memory");
    stepm(14);
    WAIT_VM(16); BARRIER(); stepm(15);
    WAIT_VM(8); WAIT_LGKM0(); BARRIER();
    asm volatile("" : "+v"(f0), "+v"(f1));
    {
      u64 d0 = (((u64)f0[1] << 32) | f0[0]) - tag, d1 = (((u64)f1[1] << 32) | f1[0]) - tag;
      if (!__all(d0 >= 1 && d0 <= 3 && d1 >= 1 && d1 <= 3)) { wait_flag(rs, tag, fo0, 1); wait_flag(rs, tag, fo1, 1); }
    }
    u32x4 pv[5];
    {
      const char* src = ws + WS_CSP + (unsigned)(ct * 16 + ld * 4) * 1024;
      const unsigned vo = (unsigned)lane * 16;
      asm volatile("global_load_dwordx4 %0, %5, %6 sc1\n\tglobal_load_dwordx4 %1, %5, %6 offset:1024 sc1\n\t"
                   "global_load_dwordx4 %2, %5, %6 offset:2048 sc1\n\tglobal_load_dwordx4 %3, %5, %6 offset:3072 sc1\n\t"
                   "global_load_dwordx4 %4, %5, %7 sc1"
                   : "=&v"(pv[0]), "=&v"(pv[1]), "=&v"(pv[2]), "=&v"(pv[3]), "=&v"(pv[4])
                   : "v"(vo), "s"(src), "s"(ws + WS_RST + (unsigned)rb * 1024) : "memory");
    }
    BARRIER();
    WAIT_VM(9); BARRIER();
    WAIT_VM(0); BARRIER();
    asm volatile("" : "+v"(pv[0]), "+v"(pv[1]), "+v"(pv[2]), "+v"(pv[3]), "+v"(pv[4]));
    {
      f32x4 v = __builtin_bit_cast(f32x4, pv[0]) + __builtin_bit_cast(f32x4, pv[1]) + __builtin_bit_cast(f32x4, pv[2]) + __builtin_bit_cast(f32x4, pv[3]);
      *(f32x4*)(smem + L_RET + ld * 1024 + lane * 16) = v;
    }
    if (ld == 0) {
      f32x4 v = __builtin_bit_cast(f32x4, pv[4]);
      if (lane >= 32) {
#pragma unroll
        for (int i = 0; i < 4; ++i) {
          float var = v[i];
          float r = __builtin_amdgcn_rsqf(var + 1e-5f);
          if (type == 2) {
            float v2 = var * r * r;
            r *= __builtin_amdgcn_rsqf(v2 + 1e-5f);
          }
          v[i] = r;
        }
      }
      *(f32x4*)(smem + L_STAT + lane * 16) = v;
    }
    WAIT_LGKM0();
    BARRIER();
    {
      const float* p4 = (const float*)(smem + L_RET) + ld * 64 + lane;
      *(float*)(smem + L_CS + (ld * 64 + lane) * 4) = (p4[0] + p4[256]) + (p4[512] + p4[768]);
    }
  }
  WAIT_LGKM0();
  BARRIER();

  const float* st_m = (const float*)(smem + L_STAT);
  const float* st_r = st_m + 128;
  const float* csum = (const float*)(smem + L_CS);
  if (type == 0) {
    const int sw = w & 3, rp = w >> 2, mg = sw >> 1, hN = sw & 1;
    char* slot = smem + ((sw * 4 + 2 * rp) * 7) * 1024 + lane * 16;
    f32x4 mv[2], rv[2];
#pragma unroll
    for (int rt = 0; rt < 2; ++rt) {
      mv[rt] = *(const f32x4*)(st_m + 64 * mg + 32 * rp + 16 * rt + 4 * g);
      rv[rt] = *(const f32x4*)(st_r + 64 * mg + 32 * rp + 16 * rt + 4 * g);
    }
    s16x8 fr[5];
#pragma unroll
    for (int u = 0; u < 5; ++u) {
      const float cc = csum[4 * c4_of(0, ch0, hN, u, l15 >> 2) + (l15 & 3)];
      unsigned pw[4];
#pragma unroll
      for (int rt = 0; rt < 2; ++rt) {
        const f32x4 a = *(const f32x4*)(slot + (rt * 7 + u) * 1024);
        float o[4];
#pragma unroll
        for (int i = 0; i < 4; ++i) {
          float v = (a[i] - mv[rt][i] * cc) * rv[rt][i];
          if (u < 2) o[i] = phi_f(v);
          else if (u == 2) { float ph = phi_f(v); o[i] = (l15 < 8) ? ph : v; }
          else o[i] = v;
        }
        pw[2 * rt] = pk_bf16(o[0], o[1]); pw[2 * rt + 1] = pk_bf16(o[2], o[3]);
      }
      fr[u] = __builtin_bit_cast(s16x8, (u32x4){pw[0], pw[1], pw[2], pw[3]});
    }
    const s16x8 ones = __builtin_bit_cast(s16x8, (u32x4){0x3F803F80u, 0x3F803F80u, 0x3F803F80u, 0x3F803F80u});
    const f32x4 z4 = (f32x4){0.f, 0.f, 0.f, 0.f};
    *(f32x4*)(slot + 0 * 1024) = MFMA16(fr[0], fr[2], z4);
    *(f32x4*)(slot + 1 * 1024) = MFMA16(fr[0], fr[3], z4);
    *(f32x4*)(slot + 2 * 1024) = MFMA16(fr[1], fr[2], z4);
    *(f32x4*)(slot + 3 * 1024) = MFMA16(fr[1], fr[3], z4);
    *(f32x4*)(slot + 4 * 1024) = MFMA16(fr[1], fr[4], z4);
    *(f32x4*)(slot + 7 * 1024) = MFMA16(fr[2], fr[3], z4);
    *(f32x4*)(slot + 8 * 1024) = MFMA16(fr[2], fr[4], z4);
    *(f32x4*)(slot + 9 * 1024) = MFMA16(fr[0], ones, z4);
    *(f32x4*)(slot + 10 * 1024) = MFMA16(fr[1], ones, z4);
    *(f32x4*)(slot + 11 * 1024) = MFMA16(fr[2], ones, z4);
    WAIT_LGKM0();
    BARRIER();
    if (tid < 420) {
      int hh = tid / 105, rem = tid - hh * 105, e = rem / 5, d4 = rem - e * 5;
      int hl = hh & 1;
      int dk = 20 * hl + 4 * d4, ua = dk >> 4, gg = (dk & 15) >> 2;
      int ev = 40 + 20 * hl + e, ub = ev >> 4;
      int tile = (e == 20) ? 7 + ua : (ua == 0) ? ub - 2 : (ua == 1) ? ub : ub + 2;
      int ln = (e == 20) ? 0 : (ev & 15);
      const char* src = smem + (((hh >> 1) * 4) * 7 + tile + (tile >= 5 ? 2 : 0)) * 1024 + (gg * 16 + ln) * 16;
      f32x4 v = *(const f32x4*)src + *(const f32x4*)(src + 14 * 1024) + *(const f32x4*)(src + 56 * 1024) + *(const f32x4*)(src + 70 * 1024);
      u32x4 vv = __builtin_bit_cast(u32x4, v);
#pragma unroll
      for (int j = 0; j < 4; ++j) if (vv[j] == SENT) vv[j] = 0x7FC00000u;
      ST16(vv, rs, WS_KVP + (unsigned)((rb * 32 + 4 * ct) * 420 + tid * 4) * 4);
    }
  }
  {
    const int mg = w >> 2, rt = w & 3;
    const float sc = st_r[16 * w + l15], mS = -st_m[16 * w + l15] * sc;
    const unsigned abase = WS_ACT + (unsigned)(rb * 8 + w) * KS_TOT * 1024;
    if (type == 2) {
      const char* dsrc = smem + ((2 * mg * 4 + rt) * 7) * 1024 + lane * 16;
      const char* csrc = (const char*)csum;
      const unsigned co = (unsigned)(2 * ch0 + g) * 16;
      const char* bsrc = smem + L_BIAS + 8 * g;
      const unsigned hbase = abase + (unsigned)(7 * (ct - 9)) * 1024 + lane * 16;
#pragma unroll
      for (int i = 0; i < 7; ++i) {
        unsigned pk[4];
#pragma unroll
        for (int hf = 0; hf < 2; ++hf) {
          const int T = 2 * i + hf;
          const f32x4 a = *(const f32x4*)(dsrc + (T + (T >= 7 ? 21 : 0)) * 1024);
          const f32x4 cc = *(const f32x4*)(csrc + ((co + T * 64) & 1023u));
          const u32x2 bb = *(const u32x2*)(bsrc + T * 32);
          const float bs[4] = {bf_lo(bb[0]), bf_hi(bb[0]), bf_lo(bb[1]), bf_hi(bb[1])};
          float o[4];
#pragma unroll
          for (int j = 0; j < 4; ++j) o[j] = gelu_f(__builtin_fmaf(a[j], sc, __builtin_fmaf(mS, cc[j], bs[j])));
          pk[2 * hf] = pk_bf16(o[0], o[1]); pk[2 * hf + 1] = pk_bf16(o[2], o[3]);
        }
        if (i < 6 || ct < 15)
          ST16(((u32x4){pk[0], pk[1], pk[2], pk[3]}), rs, hbase + (unsigned)i * 1024);
      }
    } else {
      const int nit = type == 0 ? 2 : 4;
#pragma unroll 2
      for (int i = 0; i < nit; ++i) {
        unsigned pk[4];
#pragma unroll
        for (int hf = 0; hf < 2; ++hf) {
          const int hN = type == 0 ? i : i >> 1, u = type == 0 ? 5 + hf : 2 * (i & 1) + hf;
          const f32x4 a = *(const f32x4*)(smem + (((2 * mg + hN) * 4 + rt) * 7 + u) * 1024 + lane * 16);
          const f32x4 cc = *(const f32x4*)(csum + 4 * ((type == 0 ? 48 : 0) + 8 * i + 4 * hf + g));
          float o[4];
#pragma unroll
          for (int j = 0; j < 4; ++j) o[j] = phi_f(__builtin_fmaf(a[j], sc, mS * cc[j]));
          pk[2 * hf] = pk_bf16(o[0], o[1]); pk[2 * hf + 1] = pk_bf16(o[2], o[3]);
        }
        const int ks = type == 0 ? 48 + 2 * ct + i : 64 + i;
        ST16(((u32x4){pk[0], pk[1], pk[2], pk[3]}), rs, abase + (unsigned)ks * 1024 + lane * 16);
      }
    }
  }
  {
    WAIT_VM(0);
    unsigned old = 0;
    if (lane == 0) old = __hip_atomic_fetch_add((LAS unsigned*)(smem + L_CNT), 1u, __ATOMIC_RELAXED, __HIP_MEMORY_SCOPE_WORKGROUP);
    if (__builtin_amdgcn_readfirstlane(old) == 7) post_flag(ws, rb, ct, tag + 2, lane);
  }
  if (w == 7) {
    const int rhead = bid >> 3, rdw = (bid & 7) * 27 + (lane & 31);
    float a, b;
#pragma unroll 1
    for (int spin = 0; spin < (1 << 19); ++spin) {
      u32x2 part[8];
#pragma unroll
      for (int p = 0; p < 8; ++p)
        part[p] = __builtin_amdgcn_raw_buffer_load_b64(rs, (unsigned)(min(rdw, 209) * 2 + (lane >> 5) * 32 * 420) * 4,
                                                       WS_KVP + (unsigned)((p * 64 + rhead) * 420) * 4, 16);
      bool bad = false;
      a = 0.f; b = 0.f;
#pragma unroll
      for (int p = 0; p < 8; ++p) {
        bad |= (part[p][0] == SENT) || (part[p][1] == SENT);
        a += __uint_as_float(part[p][0]); b += __uint_as_float(part[p][1]);
      }
      if (!__any(bad && (lane & 31) < 27)) break;
      __builtin_amdgcn_s_sleep(4);
    }
    a += __shfl_xor(a, 32); b += __shfl_xor(b, 32);
    unsigned v = pk_bf16(a, b);
    if (v == SENT) v = 0x7FC07FC0u;
    if (lane < 27 && rdw < 210)
      __hip_atomic_store((GAS unsigned*)(ws + WS_KVC) + rhead * 256 + rdw, v, __ATOMIC_RELAXED, __HIP_MEMORY_SCOPE_AGENT);
  }

  const int rtl = (xcd >> 1) * 8 + (idx & 7);
  const int cl = (xcd & 1) * 4 + (idx >> 3);
  {
    f32x4 acc[5][4];
#pragma unroll
    for (int n = 0; n < 5; ++n)
#pragma unroll
      for (int r = 0; r < 4; ++r) acc[n][r] = (f32x4){0.f, 0.f, 0.f, 0.f};
    if (w < 4) p2_owave(smem, ws, rs, w, lane, rtl, cl, acc);
    else p2_hwave(ws, rs, tag, w, lane, rtl, cl, acc);
    u32x2 xv[3], bv[3];
#pragma unroll
    for (int i = 0; i < 3; ++i) {
      const int tau = min(w + 8 * i, 19), n = tau >> 2, r = tau & 3;
      const int row = rtl * 64 + r * 16 + l15, col = cl * 80 + n * 16 + 4 * g;
      xv[i] = *(const u32x2*)(x + ((size_t)row * 640 + col) * 2);
      bv[i] = *(const u32x2*)(b2 + col * 2);
    }
#pragma unroll
    for (int n = 0; n < 5; ++n)
#pragma unroll
      for (int r = 0; r < 4; ++r) *(f32x4*)(smem + w * P2_REG + (n * 4 + r) * 1024 + lane * 16) = acc[n][r];
    WAIT_LGKM0();
    BARRIER();
#pragma unroll
    for (int i = 0; i < 3; ++i) {
      const int tau = w + 8 * i;
      if (tau < 20) {
        const char* src = smem + tau * 1024 + lane * 16;
        f32x4 v = *(const f32x4*)src;
#pragma unroll
        for (int sw = 1; sw < 8; ++sw) v += *(const f32x4*)(src + sw * P2_REG);
        const int n = tau >> 2, r = tau & 3;
        const int row = rtl * 64 + r * 16 + l15, col = cl * 80 + n * 16 + 4 * g;
        float o0 = v[0] + bf_lo(xv[i][0]) + bf_lo(bv[i][0]);
        float o1 = v[1] + bf_hi(xv[i][0]) + bf_hi(bv[i][0]);
        float o2 = v[2] + bf_lo(xv[i][1]) + bf_lo(bv[i][1]);
        float o3 = v[3] + bf_hi(xv[i][1]) + bf_hi(bv[i][1]);
        *(u32x2*)(out + ((size_t)row * 640 + col) * 2) = (u32x2){pk_bf16(o0, o1), pk_bf16(o2, o3)};
      }
    }
  }
  if (bid == 0 && tid == 0) __hip_atomic_store((GAS u64*)(ws + WS_EPOCH), tag, __ATOMIC_RELAXED, __HIP_MEMORY_SCOPE_AGENT);
}

}

extern "C" void kernel_launch(void* const* d_in, const int* in_sizes, int n_in,
                              void* d_out, int out_size, void* d_ws, size_t ws_size,
                              hipStream_t stream) {
  const char* x = (const char*)d_in[0];
  const char* wqkv = (const char*)d_in[1];
  const char* wo = (const char*)d_in[2];
  const char* w1 = (const char*)d_in[4];
  const char* b1 = (const char*)d_in[5];
  const char* w2 = (const char*)d_in[6];
  const char* b2 = (const char*)d_in[7];
  fused<<<256, 512, 0, stream>>>(x, wqkv, wo, w1, b1, w2, b2, (char*)d_ws, (char*)d_out);
}
```
